```python
import torch

def _softmax(x, axis=-1):
    return torch.softmax(x, dim=axis)
def _gelu(x):
    return torch.nn.functional.gelu(x, approximate="tanh")
def _silu(x):
    return torch.nn.functional.silu(x)
def _sigmoid(x):
    return torch.sigmoid(x)
def _layernorm(x, axis=-1, eps=1e-5):
    m = x.mean(dim=axis, keepdim=True)
    v = x.var(dim=axis, keepdim=True, unbiased=False)
    return (x - m) / torch.sqrt(v + eps)
def _scatter_add(tgt, idx, src, axis=0):
    return tgt.clone().index_add_(axis, idx, src)
def _segment_sum(src, seg, n):
    out = torch.zeros((n,) + src.shape[1:], dtype=src.dtype)
    return out.index_add_(0, seg, src)
def _pad(x, pw):
    flat = tuple(v for pair in reversed(pw) for v in pair)
    return torch.nn.functional.pad(x, flat)

kernel_name = 'recurrent_scan__sliding_block_cached__dense_activation_relu__decode'

def setup_inputs():

    _w = (lambda a: (lambda t: t.to(torch.bfloat16) if t.is_floating_point() else t)(torch.as_tensor(a)))
    def _A_setup():
        B, Sq, D = 2, 1, 640
        Hq, Hkv, Sk, Dh = 32, 32, 8192, 128
        assert Hq % Hkv == 0, (Hq, Hkv)
        return {
            "x": _w((lambda *s: torch.randn(*s, dtype=torch.bfloat16) * 0.05)(B, Sq, D)),
            "k_cache": _w((lambda *s: torch.randn(*s, dtype=torch.bfloat16) * 1.6)(B, Hkv, Sk, Dh)),
            "v_cache": _w((lambda *s: torch.randn(*s, dtype=torch.bfloat16) * 0.2)(B, Hkv, Sk, Dh)),
            "wq": _w((lambda *s: torch.randn(*s, dtype=torch.bfloat16) * 1.6)(D, Hq * Dh) * (D ** -0.5)),
            "wo": _w((lambda *s: torch.randn(*s, dtype=torch.bfloat16) * 0.2)(Hq * Dh, D) * ((Hq * Dh) ** -0.5)),
        }
    _A_in = _A_setup()

    def _B_setup():
        B, S, D, Dff = 2, 1, 640, 1728
        return {
            "x": _w((lambda *s: torch.randn(*s, dtype=torch.bfloat16) * 0.05)(B, S, D)),
            "w1": _w((lambda *s: torch.randn(*s, dtype=torch.bfloat16) * 0.1)(D, Dff)),
            "w2": _w((lambda *s: torch.randn(*s, dtype=torch.bfloat16) * 0.1)(Dff, D)),
        }
    _B_in = _B_setup()

    return {
        "A_x": _A_in["x"],
        "A_k_cache": _A_in["k_cache"],
        "A_v_cache": _A_in["v_cache"],
        "A_wq": _A_in["wq"],
        "A_wo": _A_in["wo"],
        "B_x": _B_in["x"],
        "B_w1": _B_in["w1"],
        "B_w2": _B_in["w2"],
    }

def reference(A_x, A_k_cache, A_v_cache, A_wq, A_wo, B_x, B_w1, B_w2):
    def _A_ref(x, k_cache, v_cache, wq, wo):
        B, Sq, D = x.shape
        Hq, Dh = 32, 128
        Hkv, Sk = k_cache.shape[1], k_cache.shape[2]
        W = min(4096, max(256, Sk // 4))
        rep = Hq // Hkv
        q = (lambda x, *d: x.permute(*d))((x @ wq).reshape(B, Sq, Hq, Dh), 0, 2, 1, 3)
        k = (lambda x, n, axis: torch.repeat_interleave(x, n, dim=axis))(k_cache[:, :, Sk - W:, :], rep, axis=1)
        v = (lambda x, n, axis: torch.repeat_interleave(x, n, dim=axis))(v_cache[:, :, Sk - W:, :], rep, axis=1)
        scores = torch.einsum("bhqd,bhkd->bhqk", q, k) * (Dh ** -0.5)
        p = _softmax(scores, axis=-1)
        o = torch.einsum("bhqk,bhkd->bhqd", p, v)
        return (lambda x, *d: x.permute(*d))(o, 0, 2, 1, 3).reshape(B, Sq, Hq * Dh) @ wo

    def _B_ref(x, w1, w2):
        h = torch.einsum("bsd,df->bsf", x, w1)
        zero = torch.zeros_like(h)
        h = torch.maximum(h, zero)
        return torch.einsum("bsf,fd->bsd", h, w2)

    x = A_x
    xn = _layernorm(x, axis=-1)
    s = _A_ref(xn, A_k_cache, A_v_cache, A_wq, A_wo)
    g = _silu(_B_ref(xn, B_w1, B_w2))
    return x + s * g
```

```cpp
#include <hip/hip_runtime.h>
#include <hip/hip_bf16.h>
#include <stdint.h>

constexpr int B = 2, D = 640, H = 32, SK = 8192, DH = 128, W = 2048, DFF = 1728;
constexpr int NSPLIT = 4, KPS = W / NSPLIT;

__device__ __forceinline__ float bf2f(unsigned short u) { return __uint_as_float((unsigned)u << 16); }
__device__ __forceinline__ float rbf(float f) { return __bfloat162float(__float2bfloat16(f)); }

__device__ void layernorm(const unsigned short* x, float* xn  , float* red) {
  int tid = threadIdx.x;
  for (int b = 0; b < B; ++b) {
    float s = 0.f;
    for (int i = tid; i < D; i += blockDim.x) s += bf2f(x[b * D + i]);
    red[tid] = s; __syncthreads();
    for (int o = blockDim.x / 2; o > 0; o >>= 1) { if (tid < o) red[tid] += red[tid + o]; __syncthreads(); }
    float mean = red[0] / D; __syncthreads();
    float v = 0.f;
    for (int i = tid; i < D; i += blockDim.x) { float d = bf2f(x[b * D + i]) - mean; v += d * d; }
    red[tid] = v; __syncthreads();
    for (int o = blockDim.x / 2; o > 0; o >>= 1) { if (tid < o) red[tid] += red[tid + o]; __syncthreads(); }
    float var = red[0] / D; __syncthreads();
    float mb = rbf(mean), vb = rbf(var);
    float den = rbf(sqrtf(rbf(vb + 1e-5f)));
    for (int i = tid; i < D; i += blockDim.x) xn[b * D + i] = rbf(rbf(bf2f(x[b * D + i]) - mb) / den);
    __syncthreads();
  }
}

__global__ void k1(const unsigned short* x, const unsigned short* wq, const unsigned short* w1, float* q, float* h) {
  __shared__ float xn[2 * D]; __shared__ float red[256];
  layernorm(x, xn, red);
  int n = blockIdx.x * 256 + threadIdx.x;
  if (n < H * DH) {
    float a0 = 0, a1 = 0;
    for (int k = 0; k < D; ++k) { float w = bf2f(wq[(size_t)k * H * DH + n]); a0 += xn[k] * w; a1 += xn[D + k] * w; }
    q[n] = rbf(a0); q[H * DH + n] = rbf(a1);
  } else if (n < H * DH + DFF) {
    int f = n - H * DH;
    float a0 = 0, a1 = 0;
    for (int k = 0; k < D; ++k) { float w = bf2f(w1[(size_t)k * DFF + f]); a0 += xn[k] * w; a1 += xn[D + k] * w; }
    h[f] = fmaxf(rbf(a0), 0.f); h[DFF + f] = fmaxf(rbf(a1), 0.f);
  }
}

__global__ void k2(const unsigned short* kc, const unsigned short* vc, const float* q, float* o) {
  __shared__ float sc[W]; __shared__ float qs[DH]; __shared__ float red[256];
  int bh = blockIdx.x, tid = threadIdx.x;
  if (tid < DH) qs[tid] = q[bh * DH + tid];
  __syncthreads();
  const unsigned short* kp = kc + ((size_t)bh * SK + (SK - W)) * DH;
  const unsigned short* vp = vc + ((size_t)bh * SK + (SK - W)) * DH;
  float mx = -INFINITY;
  for (int j = tid; j < W; j += 256) {
    float s = 0;
    for (int d = 0; d < DH; ++d) s += qs[d] * bf2f(kp[(size_t)j * DH + d]);
    s = rbf(rbf(s) * 0.08838834764831845f);
    sc[j] = s; mx = fmaxf(mx, s);
  }
  red[tid] = mx; __syncthreads();
  for (int off = 128; off > 0; off >>= 1) { if (tid < off) red[tid] = fmaxf(red[tid], red[tid + off]); __syncthreads(); }
  mx = red[0]; __syncthreads();
  float l = 0;
  for (int j = tid; j < W; j += 256) { float p = __expf(sc[j] - mx); sc[j] = p; l += p; }
  red[tid] = l; __syncthreads();
  for (int off = 128; off > 0; off >>= 1) { if (tid < off) red[tid] += red[tid + off]; __syncthreads(); }
  l = red[0]; __syncthreads();
  if (tid < DH) {
    float a = 0;
    for (int j = 0; j < W; ++j) a += sc[j] * bf2f(vp[(size_t)j * DH + tid]);
    o[bh * DH + tid] = rbf(a / l);
  }
}

__global__ void k3(const unsigned short* x, const unsigned short* wo, const unsigned short* w2, const float* o, const float* h, __hip_bfloat16* out) {
  int n = blockIdx.x * 64 + threadIdx.x; int b = blockIdx.y;
  float s = 0, f = 0;
  for (int k = 0; k < H * DH; ++k) s += o[b * H * DH + k] * bf2f(wo[(size_t)k * D + n]);
  for (int k = 0; k < DFF; ++k) f += h[b * DFF + k] * bf2f(w2[(size_t)k * D + n]);
  s = rbf(s); f = rbf(f);
  float g = rbf(f / (1.f + __expf(-f)));
  out[b * D + n] = __float2bfloat16(bf2f(x[b * D + n]) + rbf(s * g));
}

extern "C" void kernel_launch(void* const* d_in, const int* in_sizes, int n_in, void* d_out, int out_size, void* d_ws, size_t ws_size, hipStream_t stream) {
  const unsigned short* x = (const unsigned short*)d_in[0];
  const unsigned short* kc = (const unsigned short*)d_in[1];
  const unsigned short* vc = (const unsigned short*)d_in[2];
  const unsigned short* wq = (const unsigned short*)d_in[3];
  const unsigned short* wo = (const unsigned short*)d_in[4];
  const unsigned short* w1 = (const unsigned short*)d_in[6];
  const unsigned short* w2 = (const unsigned short*)d_in[7];
  float* q = (float*)d_ws; float* h = q + 2 * H * DH; float* o = h + 2 * DFF;
  k1<<<(H * DH + DFF + 255) / 256, 256, 0, stream>>>(x, wq, w1, q, h);
  k2<<<B * H, 256, 0, stream>>>(kc, vc, q, o);
  k3<<<dim3(D / 64, B), 64, 0, stream>>>(x, wo, w2, o, h, (__hip_bfloat16*)d_out);
}
```

```cpp
#include <hip/hip_runtime.h>
#include <hip/hip_bf16.h>
#include <stdint.h>

namespace {
typedef unsigned v4u __attribute__((ext_vector_type(4)));
typedef unsigned v2u __attribute__((ext_vector_type(2)));
typedef float f32x4 __attribute__((ext_vector_type(4)));
typedef float f32x2 __attribute__((ext_vector_type(2)));
typedef short s16x4 __attribute__((ext_vector_type(4)));
typedef __bf16 bf16x2 __attribute__((ext_vector_type(2)));
typedef __bf16 bf16x8 __attribute__((ext_vector_type(8)));
typedef __amdgpu_buffer_rsrc_t rsrc_t;

constexpr int D = 640, H = 32, SK = 8192, DH = 128, WIN = 2048, DFF = 1728, NG = 27;
constexpr int AUX_NT = 2, AUX_SC1 = 16, RSRC3 = 0x00020000;
constexpr int OOB = 0x7ffffff0;
constexpr int WS_EPOCH = 0;
constexpr int WS_QP = 4096;
constexpr int WS_HP = WS_QP + 32 * 8 * 2 * 1024;
constexpr int WS_AP = WS_HP + 32 * 8 * 1024;
constexpr int WS_SP = WS_AP + 32 * 8 * 1280;
constexpr int WS_FP = WS_SP + 8 * 32 * 1280;
constexpr int WS_END = WS_FP + 8 * 32 * 1280;
constexpr unsigned SPIN_CAP = 1u << 18;
#define GATHER_SLEEP 40
#define XAUX 0
#define KWIN 12
#define KPRE 2
#define KDELAY 10
#define CODE_KB 0
#define VPRE 16
#define VRING 20
#define STAMP(k) do { } while (0)
#define STAMP_FLUSH() do { } while (0)
#define NPOLL(k) do { } while (0)

#define SB() __builtin_amdgcn_sched_barrier(0)
#define MEMBAR() asm volatile("" ::: "memory")
#define VMCNT(n) asm volatile("s_waitcnt vmcnt(%0)" ::"n"(n) : "memory")
__device__ __forceinline__ void wg_barrier() { SB(); asm volatile("s_waitcnt lgkmcnt(0)\n\ts_barrier" ::: "memory"); SB(); }
__device__ __forceinline__ float bflo(unsigned u) { return __uint_as_float(u << 16); }
__device__ __forceinline__ float bfhi(unsigned u) { return __uint_as_float(u & 0xffff0000u); }
__device__ __forceinline__ unsigned cvtpk(float lo, float hi) { bf16x2 v = {(__bf16)lo, (__bf16)hi}; return __builtin_bit_cast(unsigned, v); }
__device__ __forceinline__ float rbf(float f) { return __uint_as_float(cvtpk(f, f) & 0xffff0000u); }
template <int CTRL> __device__ __forceinline__ float dppf(float x) {
  return __builtin_bit_cast(float, __builtin_amdgcn_mov_dpp(__builtin_bit_cast(int, x), CTRL, 0xf, 0xf, true));
}
template <int CTRL> __device__ __forceinline__ unsigned dppu(unsigned x) { return (unsigned)__builtin_amdgcn_mov_dpp((int)x, CTRL, 0xf, 0xf, true); }
constexpr int XOR1 = 0xB1, XOR2 = 0x4E, HMIR = 0x141, ROR8 = 0x128, ROR4 = 0x124, NEWB = 0x150;
__device__ __forceinline__ float x16_sum(float x) { auto s = __builtin_amdgcn_permlane16_swap(__float_as_uint(x), __float_as_uint(x), false, false); return __uint_as_float(s[0]) + __uint_as_float(s[1]); }
__device__ __forceinline__ float x32_sum(float x) { auto s = __builtin_amdgcn_permlane32_swap(__float_as_uint(x), __float_as_uint(x), false, false); return __uint_as_float(s[0]) + __uint_as_float(s[1]); }
__device__ __forceinline__ float x16_max(float x) { auto s = __builtin_amdgcn_permlane16_swap(__float_as_uint(x), __float_as_uint(x), false, false); return fmaxf(__uint_as_float(s[0]), __uint_as_float(s[1])); }
__device__ __forceinline__ float x32_max(float x) { auto s = __builtin_amdgcn_permlane32_swap(__float_as_uint(x), __float_as_uint(x), false, false); return fmaxf(__uint_as_float(s[0]), __uint_as_float(s[1])); }
__device__ __forceinline__ float row_sum(float x) { x += dppf<XOR1>(x); x += dppf<XOR2>(x); x += dppf<HMIR>(x); x += dppf<ROR8>(x); return x; }
__device__ __forceinline__ float row_max(float x) { x = fmaxf(x, dppf<XOR1>(x)); x = fmaxf(x, dppf<XOR2>(x)); x = fmaxf(x, dppf<HMIR>(x)); x = fmaxf(x, dppf<ROR8>(x)); return x; }
__device__ __forceinline__ float dot2(unsigned k, unsigned q, float acc) {
  return __builtin_amdgcn_fdot2_f32_bf16(__builtin_bit_cast(bf16x2, k), __builtin_bit_cast(bf16x2, q), acc, false);
}
__device__ __forceinline__ f32x4 mfma32(v4u a, v4u b, f32x4 c) {
  return __builtin_amdgcn_mfma_f32_16x16x32_bf16(__builtin_bit_cast(bf16x8, a), __builtin_bit_cast(bf16x8, b), c, 0, 0, 0);
}
__device__ __forceinline__ f32x4 mfma16(v2u a, v2u b, f32x4 c) {
  return __builtin_amdgcn_mfma_f32_16x16x16bf16_1k(__builtin_bit_cast(s16x4, a), __builtin_bit_cast(s16x4, b), c, 0, 0, 0);
}
__device__ __forceinline__ rsrc_t mkrsrc(const void* p, unsigned bytes) { return __builtin_amdgcn_make_buffer_rsrc((void*)p, 0, bytes, RSRC3); }
__device__ __forceinline__ v4u ld_nt(rsrc_t r, int voff, int soff) { return __builtin_amdgcn_raw_buffer_load_b128(r, voff, soff, AUX_NT); }
__device__ __forceinline__ v2u ld_nt64(rsrc_t r, int voff, int soff) { return __builtin_amdgcn_raw_buffer_load_b64(r, voff, soff, AUX_NT); }
__device__ __forceinline__ v4u ld_sc1(rsrc_t r, int voff, int soff) { return __builtin_amdgcn_raw_buffer_load_b128(r, voff, soff, AUX_SC1); }
__device__ __forceinline__ void st_unit(rsrc_t r, int voff, float a, float b, unsigned tag) {
  v4u v = {__float_as_uint(a), tag, __float_as_uint(b), tag};
  __builtin_amdgcn_raw_buffer_store_b128(v, r, voff, 0, AUX_SC1);
}
__device__ __forceinline__ bool unit_ok(v4u g, unsigned tag) { return (g.y == tag) & (g.w == tag); }
__device__ __forceinline__ float ux(v4u g) { return __uint_as_float(g.x); }
__device__ __forceinline__ float uz(v4u g) { return __uint_as_float(g.z); }
__device__ __forceinline__ unsigned psel(int e, int sh, int jj, bool on) {
  const unsigned pair = (unsigned)(2 * sh) | ((unsigned)(2 * sh + 1) << 8);
  const unsigned s = (e & 1) ? ((pair << 16) | 0x0c0cu) : (0x0c0c0000u | pair);
  return (on && (e >> 1) == jj) ? s : 0x0c0c0c0cu;
}
__device__ __forceinline__ unsigned perm(unsigned src, unsigned sel) { return __builtin_amdgcn_perm(src, src, sel); }
__device__ __forceinline__ v4u perm4(unsigned src, const unsigned (&s)[4]) { return v4u{perm(src, s[0]), perm(src, s[1]), perm(src, s[2]), perm(src, s[3])}; }

struct __attribute__((aligned(16))) Lds {
  f32x4 ds[4][64];
  f32x4 df[4][64];
  float acc[4][128];
  float ml[8];
  unsigned short xn[2][2][80];
  unsigned short qh[2][128];
  unsigned tag; unsigned pad[3];
};

struct XRegs { v4u c[3]; };
__device__ __forceinline__ void x_issue(XRegs& xr, rsrc_t rx, int lane) {
  const int vo = (lane >> 5) * 1280 + (lane & 31) * 16;
  xr.c[0] = __builtin_amdgcn_raw_buffer_load_b128(rx, vo, 0, XAUX);
  xr.c[1] = __builtin_amdgcn_raw_buffer_load_b128(rx, vo, 512, XAUX);
  xr.c[2] = __builtin_amdgcn_raw_buffer_load_b128(rx, (lane & 16) ? OOB : vo, 1024, XAUX);
}
__device__ __forceinline__ void ln_slice(const XRegs& xr, int lane, int i, unsigned short* dst) {
  float s = 0.f;
#pragma unroll
  for (int t = 0; t < 3; ++t)
#pragma unroll
    for (int k = 0; k < 4; ++k) s += bflo(xr.c[t][k]) + bfhi(xr.c[t][k]);
  s = x16_sum(row_sum(s));
  const float mean = s * (1.0f / D);
  float ss = 0.f, s2 = 0.f;
#pragma unroll
  for (int t = 0; t < 2; ++t)
#pragma unroll
    for (int k = 0; k < 4; ++k) { const float d0 = bflo(xr.c[t][k]) - mean, d1 = bfhi(xr.c[t][k]) - mean; ss = fmaf(d0, d0, ss); ss = fmaf(d1, d1, ss); }
#pragma unroll
  for (int k = 0; k < 4; ++k) { const float d0 = bflo(xr.c[2][k]) - mean, d1 = bfhi(xr.c[2][k]) - mean; s2 = fmaf(d0, d0, s2); s2 = fmaf(d1, d1, s2); }
  ss += (lane & 16) ? 0.f : s2;
  ss = x16_sum(row_sum(ss));
  const float mb = rbf(mean), den = rbf(sqrtf(rbf(rbf(ss * (1.0f / D)) + 1e-5f)));
  const int rel = ((lane & 31) - 10 * i) & 31;
  const int cidx = 10 * i + rel;
  const v4u m = cidx < 32 ? xr.c[0] : cidx < 64 ? xr.c[1] : xr.c[2];
  v4u o;
#pragma unroll
  for (int k = 0; k < 4; ++k) o[k] = cvtpk(rbf(bflo(m[k]) - mb) / den, rbf(bfhi(m[k]) - mb) / den);
  if (rel < 10) *reinterpret_cast<v4u*>(dst + (lane >> 5) * 80 + rel * 8) = o;
  asm volatile("s_waitcnt lgkmcnt(0)" ::: "memory");
}

__global__ void __launch_bounds__(512, 2)
fused_decode(const unsigned short* __restrict__ x, const unsigned short* __restrict__ kc, const unsigned short* __restrict__ vc,
             const unsigned short* __restrict__ wq, const unsigned short* __restrict__ wo, const unsigned short* __restrict__ w1,
             const unsigned short* __restrict__ w2, unsigned short* __restrict__ out, unsigned char* __restrict__ ws) {
  __shared__ Lds L;
  const int tid = threadIdx.x, lane = tid & 63, wave = __builtin_amdgcn_readfirstlane(tid >> 6);
  const int bid = blockIdx.x, xcd = bid & 7, sl = bid >> 3, kq = sl >> 3, h = xcd * 4 + kq, i = sl & 7, b = i >> 2, j = i & 3;
  const int gi = kq * 8 + xcd;
  const bool ffn = gi < NG;
  const int g = lane >> 4, n = lane & 15;
  const rsrc_t rws = mkrsrc(ws, WS_END);
  asm volatile("" :: "s"(x), "s"(kc), "s"(vc), "s"(wq), "s"(wo), "s"(w1), "s"(w2), "s"(out), "s"(ws));
  STAMP(0);
  const size_t kvbase = ((size_t)(b * H + h) * SK + (SK - WIN) + j * 512) * DH;
  const rsrc_t rk = mkrsrc(kc + kvbase, 131072);
  const int lo = wave * 1024 + lane * 16;
  v4u K[32];
  const int a = wave - 4;
  const bool st1 = a == 0 || (a == 1 && ffn);
  unsigned ep = 0; XRegs xr; v4u W[20];
  if (st1) {
    __builtin_amdgcn_s_setprio(3);
    const rsrc_t rx = mkrsrc(x, 2 * D * 2);
    STAMP(11);
    x_issue(xr, rx, lane);
    ep = __builtin_amdgcn_raw_buffer_load_b32(rws, WS_EPOCH, 0, AUX_SC1);
    STAMP(12);
    if (a == 0) {
      const rsrc_t rw = mkrsrc(wq, D * H * DH * 2);
      const int vo = (80 * i + 20 * g) * 8192 + h * 256 + n * 16;
#pragma unroll
      for (int t = 0; t < 20; ++t) W[t] = ld_nt(rw, vo, t * 8192);
    } else {
      const rsrc_t rw = mkrsrc(w1, D * DFF * 2);
      const int vo = (80 * i + 20 * g + (n >> 3)) * (DFF * 2) + gi * 128 + (n & 7) * 16;
#pragma unroll
      for (int u = 0; u < 10; ++u) W[u] = ld_nt(rw, vo, u * (DFF * 4));
    }
    SB();
    STAMP(10);
  } else if (wave < 4) {
#pragma unroll
    for (int t = 0; t < KPRE; ++t) K[t] = ld_nt(rk, lo, t * 4096);
  }
  int wave2 = wave;
  asm volatile("" : "+s"(wave2));
  SB();
  if (__builtin_expect(wave2 < 4, 0)) {
    const rsrc_t rv = mkrsrc(vc + kvbase, 131072);
    v4u V[32];
    STAMP(10);
    __builtin_amdgcn_s_sleep(KDELAY);
    STAMP(11);
#pragma unroll
    for (int t = KPRE; t < 32; ++t) { if (t >= KWIN) VMCNT(KWIN - 1); if (t == KWIN) STAMP(12); if (t == 31) STAMP(13); K[t] = ld_nt(rk, lo, t * 4096); SB(); }
#pragma unroll
    for (int t = 0; t < VPRE; ++t) { VMCNT(KWIN - 1); V[t] = ld_nt(rv, lo, t * 4096); SB(); }
    unsigned sel[4];
#pragma unroll
    for (int jj = 0; jj < 4; ++jj) sel[jj] = psel(n & 7, n >> 3, jj, true);
    STAMP(1);
    wg_barrier();
    STAMP(2);
#pragma unroll
    for (int t = VPRE; t < VRING; ++t) { V[t] = ld_nt(rv, lo, t * 4096); SB(); }
    const v4u qv = *reinterpret_cast<const v4u*>(&L.qh[0][n * 8]);
    const unsigned tag = L.tag;
    float xs[32];
#pragma unroll
    for (int t = 0; t < 32; ++t) { float d = dot2(K[t].x, qv.x, 0.f); d = dot2(K[t].y, qv.y, d); d = dot2(K[t].z, qv.z, d); xs[t] = dot2(K[t].w, qv.w, d); }
#define TR_STEP(HALF, CTRL, BIT) _Pragma("unroll") for (int u = 0; u < (HALF); ++u) { \
      const float lo_ = xs[u], hi_ = xs[u + (HALF)]; const float t1_ = lo_ + dppf<CTRL>(lo_), t2_ = hi_ + dppf<CTRL>(hi_); xs[u] = (n & (BIT)) ? t2_ : t1_; }
    TR_STEP(16, ROR8, 8) TR_STEP(8, HMIR, 4) TR_STEP(4, XOR2, 2) TR_STEP(2, XOR1, 1)
#undef TR_STEP
    const float s0 = rbf(rbf(xs[0]) * 0.08838834764831845f), s1 = rbf(rbf(xs[1]) * 0.08838834764831845f);
    const float m = x32_max(x16_max(row_max(fmaxf(s0, s1))));
    const float p0 = __builtin_amdgcn_exp2f((s0 - m) * 1.4426950408889634f), p1 = __builtin_amdgcn_exp2f((s1 - m) * 1.4426950408889634f);
    const unsigned P0 = cvtpk(p0, p0 - rbf(p0)), P1 = cvtpk(p1, p1 - rbf(p1));
    const float lsum = x32_sum(x16_sum(row_sum(p0 + p1)));
    STAMP(3);
    f32x4 acc = {0.f, 0.f, 0.f, 0.f};
#define PV(t) { const unsigned bc = dppu<NEWB + ((t) >> 1)>(((t) & 1) ? P1 : P0); acc = mfma32(V[t], perm4(bc, sel), acc); \
      if ((t) + VRING < 32) V[((t) + VRING) & 31] = ld_nt(rv, lo, ((t) + VRING) * 4096); SB(); \
      if ((t) + VRING == 31) { STAMP(4); wg_barrier(); } }
    PV(0) PV(1) PV(2) PV(3) PV(4) PV(5) PV(6) PV(7) PV(8) PV(9) PV(10) PV(11) PV(12) PV(13) PV(14) PV(15)
    PV(16) PV(17) PV(18) PV(19) PV(20) PV(21) PV(22) PV(23) PV(24) PV(25) PV(26) PV(27) PV(28) PV(29) PV(30) PV(31)
#undef PV
#pragma unroll
    for (int k = 0; k < 4; ++k) acc[k] += dppf<ROR8>(acc[k]);
    if (n < 8) *reinterpret_cast<f32x4*>(&L.acc[wave][(g * 8 + n) * 4]) = acc;
    if (lane == 0) { L.ml[wave] = m; L.ml[4 + wave] = lsum; }
    STAMP(5);
    wg_barrier();
    STAMP(6);
    if (wave == 0) {
      const f32x4 mm = *reinterpret_cast<const f32x4*>(&L.ml[0]), ll = *reinterpret_cast<const f32x4*>(&L.ml[4]);
      f32x2 a[4];
#pragma unroll
      for (int w = 0; w < 4; ++w) a[w] = *reinterpret_cast<const f32x2*>(&L.acc[w][2 * lane]);
      const float M = fmaxf(fmaxf(mm[0], mm[1]), fmaxf(mm[2], mm[3]));
      float v0 = 0.f, v1 = 0.f, Ls = 0.f;
#pragma unroll
      for (int w = 0; w < 4; ++w) { const float f = __builtin_amdgcn_exp2f((mm[w] - M) * 1.4426950408889634f); v0 = fmaf(f, a[w][0], v0); v1 = fmaf(f, a[w][1], v1); Ls = fmaf(f, ll[w], Ls); }
      const int ap = WS_AP + (h * 8 + i) * 1280;
      st_unit(rws, ap + lane * 16, v0, v1, tag);
      if (lane == 0) st_unit(rws, ap + 1024, M, Ls, tag);
    }
    STAMP(7);
    wg_barrier();
    STAMP(8);
    {
      const int kind = tid >> 7, col = tid & 127;
      if (col < 80 && (kind == 0 || ffn)) {
        const float* src = reinterpret_cast<const float*>(kind ? &L.df[0][0] : &L.ds[0][0]) + ((col >> 5) * 16 + (col & 7)) * 4 + ((col >> 3) & 3);
        float v0 = 0.f, v1 = 0.f;
#pragma unroll
        for (int a = 0; a < 4; ++a) { v0 += src[a * 256]; v1 += src[a * 256 + 32]; }
        st_unit(rws, (kind ? WS_FP + (i * 32 + gi) * 1280 : WS_SP + (i * 32 + h) * 1280) + col * 16, v0, v1, tag);
      }
    }
    STAMP(9);
    STAMP_FLUSH();
  } else {
    __builtin_amdgcn_s_setprio(3);
    unsigned selE[4], selO[4];
#pragma unroll
    for (int jj = 0; jj < 4; ++jj) { selE[jj] = psel(n & 7, 0, jj, true); selO[jj] = psel(n & 7, 1, jj, true); }
    if (st1) {
      const unsigned tag = ep + 1u;
      ln_slice(xr, lane, i, &L.xn[a][0][0]);
      v2u xw[5];
#pragma unroll
      for (int u = 0; u < 5; ++u) xw[u] = *reinterpret_cast<const v2u*>(&L.xn[a][n >> 3][20 * g + 4 * u]);
      STAMP(1);
      f32x4 dq = {0.f, 0.f, 0.f, 0.f};
      if (a == 0) {
#pragma unroll
        for (int t = 0; t < 20; ++t) { const unsigned src = (t & 2) ? xw[t >> 2].y : xw[t >> 2].x; dq = mfma32(W[t], perm4(src, (t & 1) ? selO : selE), dq); }
      } else {
        f32x4 d1 = {0.f, 0.f, 0.f, 0.f};
#pragma unroll
        for (int u = 0; u < 10; ++u) { const unsigned src = (u & 1) ? xw[u >> 1].y : xw[u >> 1].x; dq = mfma32(W[u], perm4(src, selE), dq); d1 = mfma32(W[u], perm4(src, selO), d1); }
#pragma unroll
        for (int k = 0; k < 4; ++k) dq[k] = x32_sum(g < 2 ? dq[k] : d1[k]);
      }
      const int po = a ? WS_HP + (gi * 8 + i) * 1024 + (n >> 3) * 512 : WS_QP + ((h * 8 + i) * 2 + (n >> 3)) * 1024;
      if (!a || g < 2) { const int qo = po + (g * 8 + (n & 7)) * 32; st_unit(rws, qo, dq[0], dq[1], tag); st_unit(rws, qo + 16, dq[2], dq[3], tag); }
      STAMP(2);
      __builtin_amdgcn_s_sleep(GATHER_SLEEP);
      const int go = (a ? WS_HP + gi * 8192 : WS_QP + (h * 16 + b) * 1024) + lane * 16, gs = a ? 1024 : 2048;
      v4u G[8];
      for (unsigned spin = 0;; ++spin) {
        MEMBAR();
#pragma unroll
        for (int p = 0; p < 8; ++p) G[p] = ld_sc1(rws, go, p * gs);
        bool ok = true;
#pragma unroll
        for (int p = 0; p < 8; ++p) ok &= unit_ok(G[p], tag);
        NPOLL(0);
        if (__all(ok) || spin > SPIN_CAP) break;
        __builtin_amdgcn_s_sleep(8);
      }
      float q0 = 0.f, q1 = 0.f;
#pragma unroll
      for (int p = 0; p < 8; ++p) { q0 += ux(G[p]); q1 += uz(G[p]); }
      if (a) { q0 = fmaxf(q0, 0.f); q1 = fmaxf(q1, 0.f); }
      const unsigned qq = cvtpk(q0, q1);
      const int d0 = 32 * g + 16 * (lane & 1) + ((lane >> 1) & 7);
      L.qh[a][d0] = (unsigned short)qq; L.qh[a][d0 + 8] = (unsigned short)(qq >> 16);
      if (tid == 256) L.tag = tag;
      STAMP(3);
    }
    wg_barrier();
    const unsigned tag = L.tag;
    const v2u hw = *reinterpret_cast<const v2u*>(&L.qh[1][(n >> 3) * 64 + 16 * a + 4 * g]);
    unsigned xres = 0;
    if (a == 0 && h < 20 && lane < 8) xres = x[(lane >> 2) * D + i * 80 + h * 4 + (lane & 3)];
    wg_barrier();
    STAMP(4);
    const rsrc_t r2 = mkrsrc(w2, DFF * D * 2), ro = mkrsrc(wo, H * DH * D * 2);
    v4u W2[4], WO[8];
    {
      const int v2o = (n < 10 && ffn) ? (64 * gi + 16 * a + 4 * g) * 1280 + i * 160 + n * 16 : OOB;
      const int voo = n < 10 ? (128 * h + 32 * a + 2 * g) * 1280 + i * 160 + n * 16 : OOB;
#pragma unroll
      for (int t = 0; t < 4; ++t) W2[t] = ld_nt(r2, v2o, t * 1280);
#pragma unroll
      for (int t = 0; t < 8; ++t) WO[t] = ld_nt(ro, voo, (8 * (t & 3) + (t >> 2)) * 1280);
      SB();
    }
    wg_barrier();
    f32x4 df = {0.f, 0.f, 0.f, 0.f};
    df = mfma32(W2[0], perm4(hw.x, selE), df); df = mfma32(W2[1], perm4(hw.x, selO), df);
    df = mfma32(W2[2], perm4(hw.y, selE), df); df = mfma32(W2[3], perm4(hw.y, selO), df);
    L.df[a][lane] = df;
    STAMP(5);
    v4u A0, A1, AM;
    {
      const int po = WS_AP + (h * 8 + (n >> 3) * 4 + (n & 3)) * 1280;
      const int uo = po + (16 * a + 4 * g + 2 * ((n >> 2) & 1)) * 16;
      for (unsigned spin = 0;; ++spin) {
        MEMBAR();
        A0 = ld_sc1(rws, uo, 0); A1 = ld_sc1(rws, uo, 16); AM = ld_sc1(rws, po, 1024);
        NPOLL(1);
        if (__all(unit_ok(A0, tag) & unit_ok(A1, tag) & unit_ok(AM, tag)) || spin > SPIN_CAP) break;
        __builtin_amdgcn_s_sleep(2);
      }
    }
    STAMP(6);
    {
      float M = ux(AM); M = fmaxf(M, dppf<XOR1>(M)); M = fmaxf(M, dppf<XOR2>(M));
      const float w = __builtin_amdgcn_exp2f((ux(AM) - M) * 1.4426950408889634f);
      float Ls = w * uz(AM); Ls += dppf<XOR1>(Ls); Ls += dppf<XOR2>(Ls);
      const float wn = w / Ls;
      float o0 = wn * ux(A0), o1 = wn * uz(A0), o2 = wn * ux(A1), o3 = wn * uz(A1);
      o0 += dppf<XOR1>(o0); o1 += dppf<XOR1>(o1); o2 += dppf<XOR1>(o2); o3 += dppf<XOR1>(o3);
      o0 += dppf<XOR2>(o0); o1 += dppf<XOR2>(o1); o2 += dppf<XOR2>(o2); o3 += dppf<XOR2>(o3);
      const unsigned m01 = cvtpk(o0, o1), m23 = cvtpk(o2, o3), t01 = dppu<HMIR>(m01), t23 = dppu<HMIR>(m23);
      const bool hf = (n & 4) != 0;
      const unsigned ow[4] = {hf ? t01 : m01, hf ? t23 : m23, hf ? m01 : t01, hf ? m23 : t23};
      f32x4 ds = {0.f, 0.f, 0.f, 0.f};
#pragma unroll
      for (int t = 0; t < 8; ++t) ds = mfma32(WO[t], perm4(ow[t >> 1], (t & 1) ? selO : selE), ds);
      L.ds[a][lane] = ds;
    }
    STAMP(7);
    wg_barrier();
    if (a == 0 && h < 20) {
      const int p = lane >> 2, co = (h * 4 + (lane & 3)) * 16;
      const int so = WS_SP + (i * 32 + p) * 1280 + co, fo = WS_FP + (i * 32 + p) * 1280 + co;
      const bool f2 = p + 16 < NG;
      v4u S0, S1, F0, F1;
      for (unsigned spin = 0;; ++spin) {
        MEMBAR();
        S0 = ld_sc1(rws, so, 0); S1 = ld_sc1(rws, so, 16 * 1280); F0 = ld_sc1(rws, fo, 0); F1 = ld_sc1(rws, f2 ? fo : OOB, 16 * 1280);
        NPOLL(2);
        if (__all(unit_ok(S0, tag) & unit_ok(S1, tag) & unit_ok(F0, tag) & (!f2 | unit_ok(F1, tag))) || spin > SPIN_CAP) break;
        __builtin_amdgcn_s_sleep(2);
      }
      STAMP(8);
      float r[4] = {ux(S0) + ux(S1), uz(S0) + uz(S1), ux(F0) + ux(F1), uz(F0) + uz(F1)};
#pragma unroll
      for (int k = 0; k < 4; ++k) { float t = r[k]; t += dppf<ROR8>(t); t += dppf<ROR4>(t); r[k] = x32_sum(x16_sum(t)); }
      const float sv = rbf((lane & 4) ? r[1] : r[0]), fv = rbf((lane & 4) ? r[3] : r[2]);
      const float gv = rbf(fv / (1.0f + __expf(-fv)));
      const float o = bflo(xres) + rbf(sv * gv);
      if (lane < 8) out[(lane >> 2) * D + i * 80 + h * 4 + (lane & 3)] = (unsigned short)cvtpk(o, o);
      if (bid == 0 && lane == 0) __builtin_amdgcn_raw_buffer_store_b32(tag, rws, WS_EPOCH, 0, AUX_SC1);
      STAMP(9);
    }
    STAMP_FLUSH();
  }
}
}

extern "C" void kernel_launch(void* const* d_in, const int* in_sizes, int n_in, void* d_out, int out_size, void* d_ws, size_t ws_size, hipStream_t stream) {
  fused_decode<<<256, 512, 0, stream>>>((const unsigned short*)d_in[0], (const unsigned short*)d_in[1], (const unsigned short*)d_in[2],
                                        (const unsigned short*)d_in[3], (const unsigned short*)d_in[4], (const unsigned short*)d_in[6],
                                        (const unsigned short*)d_in[7], (unsigned short*)d_out, (unsigned char*)d_ws);
}
```

```cpp
#include <hip/hip_runtime.h>
#include <hip/hip_bf16.h>
#include <stdint.h>

namespace {
typedef unsigned v4u __attribute__((ext_vector_type(4)));
typedef unsigned v2u __attribute__((ext_vector_type(2)));
typedef float f32x4 __attribute__((ext_vector_type(4)));
typedef float f32x2 __attribute__((ext_vector_type(2)));
typedef short s16x4 __attribute__((ext_vector_type(4)));
typedef __bf16 bf16x2 __attribute__((ext_vector_type(2)));
typedef __bf16 bf16x8 __attribute__((ext_vector_type(8)));
typedef __amdgpu_buffer_rsrc_t rsrc_t;

constexpr int D = 640, H = 32, SK = 8192, DH = 128, WIN = 2048, DFF = 1728, NG = 27;
constexpr int AUX_NT = 2, AUX_SC1 = 16, RSRC3 = 0x00020000;
constexpr int OOB = 0x7ffffff0;
constexpr int WS_EPOCH = 0;
constexpr int WS_QP = 4096;
constexpr int WS_HP = WS_QP + 32 * 8 * 2 * 1024;
constexpr int WS_AP = WS_HP + 32 * 8 * 1024;
constexpr int WS_SP = WS_AP + 32 * 8 * 1280;
constexpr int WS_FP = WS_SP + 8 * 32 * 1280;
constexpr int WS_END = WS_FP + 8 * 32 * 1280;
constexpr unsigned SPIN_CAP = 1u << 18;
#define GATHER_SLEEP 40
#define XAUX 0
#define KWIN 12
#define KPRE 2
#define KDELAY 10
#define CODE_LINES 104
#define VPRE 16
#define VRING 20
#define STAMP(k) do { } while (0)
#define STAMP_FLUSH() do { } while (0)
#define NPOLL(k) do { } while (0)

#define SB() __builtin_amdgcn_sched_barrier(0)
#define MEMBAR() asm volatile("" ::: "memory")
#define VMCNT(n) asm volatile("s_waitcnt vmcnt(%0)" ::"n"(n) : "memory")
__device__ __forceinline__ void wg_barrier() { SB(); asm volatile("s_waitcnt lgkmcnt(0)\n\ts_barrier" ::: "memory"); SB(); }
__device__ __forceinline__ float bflo(unsigned u) { return __uint_as_float(u << 16); }
__device__ __forceinline__ float bfhi(unsigned u) { return __uint_as_float(u & 0xffff0000u); }
__device__ __forceinline__ unsigned cvtpk(float lo, float hi) { bf16x2 v = {(__bf16)lo, (__bf16)hi}; return __builtin_bit_cast(unsigned, v); }
__device__ __forceinline__ float rbf(float f) { return __uint_as_float(cvtpk(f, f) & 0xffff0000u); }
template <int CTRL> __device__ __forceinline__ float dppf(float x) {
  return __builtin_bit_cast(float, __builtin_amdgcn_mov_dpp(__builtin_bit_cast(int, x), CTRL, 0xf, 0xf, true));
}
template <int CTRL> __device__ __forceinline__ unsigned dppu(unsigned x) { return (unsigned)__builtin_amdgcn_mov_dpp((int)x, CTRL, 0xf, 0xf, true); }
constexpr int XOR1 = 0xB1, XOR2 = 0x4E, HMIR = 0x141, ROR8 = 0x128, ROR4 = 0x124, NEWB = 0x150;
__device__ __forceinline__ float x16_sum(float x) { auto s = __builtin_amdgcn_permlane16_swap(__float_as_uint(x), __float_as_uint(x), false, false); return __uint_as_float(s[0]) + __uint_as_float(s[1]); }
__device__ __forceinline__ float x32_sum(float x) { auto s = __builtin_amdgcn_permlane32_swap(__float_as_uint(x), __float_as_uint(x), false, false); return __uint_as_float(s[0]) + __uint_as_float(s[1]); }
__device__ __forceinline__ float x16_max(float x) { auto s = __builtin_amdgcn_permlane16_swap(__float_as_uint(x), __float_as_uint(x), false, false); return fmaxf(__uint_as_float(s[0]), __uint_as_float(s[1])); }
__device__ __forceinline__ float x32_max(float x) { auto s = __builtin_amdgcn_permlane32_swap(__float_as_uint(x), __float_as_uint(x), false, false); return fmaxf(__uint_as_float(s[0]), __uint_as_float(s[1])); }
__device__ __forceinline__ float row_sum(float x) { x += dppf<XOR1>(x); x += dppf<XOR2>(x); x += dppf<HMIR>(x); x += dppf<ROR8>(x); return x; }
__device__ __forceinline__ float row_max(float x) { x = fmaxf(x, dppf<XOR1>(x)); x = fmaxf(x, dppf<XOR2>(x)); x = fmaxf(x, dppf<HMIR>(x)); x = fmaxf(x, dppf<ROR8>(x)); return x; }
__device__ __forceinline__ float dot2(unsigned k, unsigned q, float acc) {
  return __builtin_amdgcn_fdot2_f32_bf16(__builtin_bit_cast(bf16x2, k), __builtin_bit_cast(bf16x2, q), acc, false);
}
__device__ __forceinline__ f32x4 mfma32(v4u a, v4u b, f32x4 c) {
  return __builtin_amdgcn_mfma_f32_16x16x32_bf16(__builtin_bit_cast(bf16x8, a), __builtin_bit_cast(bf16x8, b), c, 0, 0, 0);
}
__device__ __forceinline__ f32x4 mfma16(v2u a, v2u b, f32x4 c) {
  return __builtin_amdgcn_mfma_f32_16x16x16bf16_1k(__builtin_bit_cast(s16x4, a), __builtin_bit_cast(s16x4, b), c, 0, 0, 0);
}
__device__ __forceinline__ rsrc_t mkrsrc(const void* p, unsigned bytes) { return __builtin_amdgcn_make_buffer_rsrc((void*)p, 0, bytes, RSRC3); }
__device__ __forceinline__ v4u ld_nt(rsrc_t r, int voff, int soff) { return __builtin_amdgcn_raw_buffer_load_b128(r, voff, soff, AUX_NT); }
__device__ __forceinline__ v2u ld_nt64(rsrc_t r, int voff, int soff) { return __builtin_amdgcn_raw_buffer_load_b64(r, voff, soff, AUX_NT); }
__device__ __forceinline__ v4u ld_sc1(rsrc_t r, int voff, int soff) { return __builtin_amdgcn_raw_buffer_load_b128(r, voff, soff, AUX_SC1); }
__device__ __forceinline__ void st_unit(rsrc_t r, int voff, float a, float b, unsigned tag) {
  v4u v = {__float_as_uint(a), tag, __float_as_uint(b), tag};
  __builtin_amdgcn_raw_buffer_store_b128(v, r, voff, 0, AUX_SC1);
}
__device__ __forceinline__ bool unit_ok(v4u g, unsigned tag) { return (g.y == tag) & (g.w == tag); }
__device__ __forceinline__ float ux(v4u g) { return __uint_as_float(g.x); }
__device__ __forceinline__ float uz(v4u g) { return __uint_as_float(g.z); }
__device__ __forceinline__ unsigned psel(int e, int sh, int jj, bool on) {
  const unsigned pair = (unsigned)(2 * sh) | ((unsigned)(2 * sh + 1) << 8);
  const unsigned s = (e & 1) ? ((pair << 16) | 0x0c0cu) : (0x0c0c0000u | pair);
  return (on && (e >> 1) == jj) ? s : 0x0c0c0c0cu;
}
__device__ __forceinline__ unsigned perm(unsigned src, unsigned sel) { return __builtin_amdgcn_perm(src, src, sel); }
__device__ __forceinline__ v4u perm4(unsigned src, const unsigned (&s)[4]) { return v4u{perm(src, s[0]), perm(src, s[1]), perm(src, s[2]), perm(src, s[3])}; }

struct __attribute__((aligned(16))) Lds {
  f32x4 ds[4][64];
  f32x4 df[4][64];
  float acc[4][128];
  float ml[8];
  unsigned short xn[2][2][80];
  unsigned short qh[2][128];
  unsigned tag; unsigned pad[3];
};

struct XRegs { v4u c[3]; };
__device__ __forceinline__ void x_issue(XRegs& xr, rsrc_t rx, int lane) {
  const int vo = (lane >> 5) * 1280 + (lane & 31) * 16;
  xr.c[0] = __builtin_amdgcn_raw_buffer_load_b128(rx, vo, 0, XAUX);
  xr.c[1] = __builtin_amdgcn_raw_buffer_load_b128(rx, vo, 512, XAUX);
  xr.c[2] = __builtin_amdgcn_raw_buffer_load_b128(rx, (lane & 16) ? OOB : vo, 1024, XAUX);
}
__device__ __forceinline__ void ln_slice(const XRegs& xr, int lane, int i, unsigned short* dst) {
  float s = 0.f;
#pragma unroll
  for (int t = 0; t < 3; ++t)
#pragma unroll
    for (int k = 0; k < 4; ++k) s += bflo(xr.c[t][k]) + bfhi(xr.c[t][k]);
  s = x16_sum(row_sum(s));
  const float mean = s * (1.0f / D);
  float ss = 0.f, s2 = 0.f;
#pragma unroll
  for (int t = 0; t < 2; ++t)
#pragma unroll
    for (int k = 0; k < 4; ++k) { const float d0 = bflo(xr.c[t][k]) - mean, d1 = bfhi(xr.c[t][k]) - mean; ss = fmaf(d0, d0, ss); ss = fmaf(d1, d1, ss); }
#pragma unroll
  for (int k = 0; k < 4; ++k) { const float d0 = bflo(xr.c[2][k]) - mean, d1 = bfhi(xr.c[2][k]) - mean; s2 = fmaf(d0, d0, s2); s2 = fmaf(d1, d1, s2); }
  ss += (lane & 16) ? 0.f : s2;
  ss = x16_sum(row_sum(ss));
  const float mb = rbf(mean), den = rbf(sqrtf(rbf(rbf(ss * (1.0f / D)) + 1e-5f)));
  const int rel = ((lane & 31) - 10 * i) & 31;
  const int cidx = 10 * i + rel;
  const v4u m = cidx < 32 ? xr.c[0] : cidx < 64 ? xr.c[1] : xr.c[2];
  v4u o;
#pragma unroll
  for (int k = 0; k < 4; ++k) o[k] = cvtpk(rbf(bflo(m[k]) - mb) / den, rbf(bfhi(m[k]) - mb) / den);
  if (rel < 10) *reinterpret_cast<v4u*>(dst + (lane >> 5) * 80 + rel * 8) = o;
  asm volatile("s_waitcnt lgkmcnt(0)" ::: "memory");
}

__global__ void __launch_bounds__(512, 2) __attribute__((aligned(4096)))
fused_decode(const unsigned short* __restrict__ x, const unsigned short* __restrict__ kc, const unsigned short* __restrict__ vc,
             const unsigned short* __restrict__ wq, const unsigned short* __restrict__ wo, const unsigned short* __restrict__ w1,
             const unsigned short* __restrict__ w2, unsigned short* __restrict__ out, unsigned char* __restrict__ ws) {
  __shared__ Lds L;
  const int tid = threadIdx.x, lane = tid & 63, wave = __builtin_amdgcn_readfirstlane(tid >> 6);
  const int bid = blockIdx.x, xcd = bid & 7, sl = bid >> 3, kq = sl >> 3, h = xcd * 4 + kq, i = sl & 7, b = i >> 2, j = i & 3;
  const int gi = kq * 8 + xcd;
  const bool ffn = gi < NG;
  const int g = lane >> 4, n = lane & 15;
  const rsrc_t rws = mkrsrc(ws, WS_END);
  asm volatile("" :: "s"(x), "s"(kc), "s"(vc), "s"(wq), "s"(wo), "s"(w1), "s"(w2), "s"(out), "s"(ws));
  STAMP(0);
  const size_t kvbase = ((size_t)(b * H + h) * SK + (SK - WIN) + j * 512) * DH;
  const rsrc_t rk = mkrsrc(kc + kvbase, 131072);
  const int lo = wave * 1024 + lane * 16;
  v4u K[32];
  const int a = wave - 4;
  const bool st1 = a == 0 || (a == 1 && ffn);
  unsigned ep = 0; XRegs xr; v4u W[20];
  if (st1) {
    __builtin_amdgcn_s_setprio(3);
    const rsrc_t rx = mkrsrc(x, 2 * D * 2);
    STAMP(11);
    x_issue(xr, rx, lane);
    ep = __builtin_amdgcn_raw_buffer_load_b32(rws, WS_EPOCH, 0, AUX_SC1);
    STAMP(12);
    if (a == 0) {
      const rsrc_t rw = mkrsrc(wq, D * H * DH * 2);
      const int vo = (80 * i + 20 * g) * 8192 + h * 256 + n * 16;
#pragma unroll
      for (int t = 0; t < 20; ++t) W[t] = ld_nt(rw, vo, t * 8192);
    } else {
      const rsrc_t rw = mkrsrc(w1, D * DFF * 2);
      const int vo = (80 * i + 20 * g + (n >> 3)) * (DFF * 2) + gi * 128 + (n & 7) * 16;
#pragma unroll
      for (int u = 0; u < 10; ++u) W[u] = ld_nt(rw, vo, u * (DFF * 4));
    }
    SB();
    STAMP(10);
  } else if (wave < 4) {
#pragma unroll
    for (int t = 0; t < KPRE; ++t) K[t] = ld_nt(rk, lo, t * 4096);
  }
  else if (a >= 2) {
    unsigned long long pc;
    asm volatile("s_getpc_b64 %0" : "=s"(pc));
    pc &= ~4095ull;
    const int l0 = sl * 2 + (a - 2), l1 = l0 + 64 < CODE_LINES ? l0 + 64 : l0;
    const unsigned long long p0 = pc + (unsigned)(CODE_LINES - 1 - l0) * 128u, p1 = pc + (unsigned)(CODE_LINES - 1 - l1) * 128u;
    unsigned d0, d1;
    asm volatile("s_load_dword %0, %2, 0x0\n\ts_load_dword %1, %3, 0x0\n\ts_waitcnt lgkmcnt(0)" : "=&s"(d0), "=&s"(d1) : "s"(p0), "s"(p1));
  }
  int wave2 = wave;
  asm volatile("" : "+s"(wave2));
  SB();
  if (__builtin_expect(wave2 < 4, 1)) {
    const rsrc_t rv = mkrsrc(vc + kvbase, 131072);
    v4u V[32];
    STAMP(10);
    __builtin_amdgcn_s_sleep(KDELAY);
    STAMP(11);
#pragma unroll
    for (int t = KPRE; t < 32; ++t) { if (t >= KWIN) VMCNT(KWIN - 1); if (t == KWIN) STAMP(12); if (t == 31) STAMP(13); K[t] = ld_nt(rk, lo, t * 4096); SB(); }
#pragma unroll
    for (int t = 0; t < VPRE; ++t) { VMCNT(KWIN - 1); V[t] = ld_nt(rv, lo, t * 4096); SB(); }
    unsigned sel[4];
#pragma unroll
    for (int jj = 0; jj < 4; ++jj) sel[jj] = psel(n & 7, n >> 3, jj, true);
    STAMP(1);
    wg_barrier();
    STAMP(2);
#pragma unroll
    for (int t = VPRE; t < VRING; ++t) { V[t] = ld_nt(rv, lo, t * 4096); SB(); }
    const v4u qv = *reinterpret_cast<const v4u*>(&L.qh[0][n * 8]);
    const unsigned tag = L.tag;
    float xs[32];
#pragma unroll
    for (int t = 0; t < 32; ++t) { float d = dot2(K[t].x, qv.x, 0.f); d = dot2(K[t].y, qv.y, d); d = dot2(K[t].z, qv.z, d); xs[t] = dot2(K[t].w, qv.w, d); }
#define TR_STEP(HALF, CTRL, BIT) _Pragma("unroll") for (int u = 0; u < (HALF); ++u) { \
      const float lo_ = xs[u], hi_ = xs[u + (HALF)]; const float t1_ = lo_ + dppf<CTRL>(lo_), t2_ = hi_ + dppf<CTRL>(hi_); xs[u] = (n & (BIT)) ? t2_ : t1_; }
    TR_STEP(16, ROR8, 8) TR_STEP(8, HMIR, 4) TR_STEP(4, XOR2, 2) TR_STEP(2, XOR1, 1)
#undef TR_STEP
    const float s0 = rbf(rbf(xs[0]) * 0.08838834764831845f), s1 = rbf(rbf(xs[1]) * 0.08838834764831845f);
    const float m = x32_max(x16_max(row_max(fmaxf(s0, s1))));
    const float p0 = __builtin_amdgcn_exp2f((s0 - m) * 1.4426950408889634f), p1 = __builtin_amdgcn_exp2f((s1 - m) * 1.4426950408889634f);
    const unsigned P0 = cvtpk(p0, p0 - rbf(p0)), P1 = cvtpk(p1, p1 - rbf(p1));
    const float lsum = x32_sum(x16_sum(row_sum(p0 + p1)));
    STAMP(3);
    f32x4 acc = {0.f, 0.f, 0.f, 0.f};
#define PV(t) { const unsigned bc = dppu<NEWB + ((t) >> 1)>(((t) & 1) ? P1 : P0); acc = mfma32(V[t], perm4(bc, sel), acc); \
      if ((t) + VRING < 32) V[((t) + VRING) & 31] = ld_nt(rv, lo, ((t) + VRING) * 4096); SB(); \
      if ((t) + VRING == 31) { STAMP(4); wg_barrier(); } }
    PV(0) PV(1) PV(2) PV(3) PV(4) PV(5) PV(6) PV(7) PV(8) PV(9) PV(10) PV(11) PV(12) PV(13) PV(14) PV(15)
    PV(16) PV(17) PV(18) PV(19) PV(20) PV(21) PV(22) PV(23) PV(24) PV(25) PV(26) PV(27) PV(28) PV(29) PV(30) PV(31)
#undef PV
#pragma unroll
    for (int k = 0; k < 4; ++k) acc[k] += dppf<ROR8>(acc[k]);
    if (n < 8) *reinterpret_cast<f32x4*>(&L.acc[wave][(g * 8 + n) * 4]) = acc;
    if (lane == 0) { L.ml[wave] = m; L.ml[4 + wave] = lsum; }
    STAMP(5);
    wg_barrier();
    STAMP(6);
    if (wave == 0) {
      const f32x4 mm = *reinterpret_cast<const f32x4*>(&L.ml[0]), ll = *reinterpret_cast<const f32x4*>(&L.ml[4]);
      f32x2 a[4];
#pragma unroll
      for (int w = 0; w < 4; ++w) a[w] = *reinterpret_cast<const f32x2*>(&L.acc[w][2 * lane]);
      const float M = fmaxf(fmaxf(mm[0], mm[1]), fmaxf(mm[2], mm[3]));
      float v0 = 0.f, v1 = 0.f, Ls = 0.f;
#pragma unroll
      for (int w = 0; w < 4; ++w) { const float f = __builtin_amdgcn_exp2f((mm[w] - M) * 1.4426950408889634f); v0 = fmaf(f, a[w][0], v0); v1 = fmaf(f, a[w][1], v1); Ls = fmaf(f, ll[w], Ls); }
      const int ap = WS_AP + (h * 8 + i) * 1280;
      st_unit(rws, ap + lane * 16, v0, v1, tag);
      if (lane == 0) st_unit(rws, ap + 1024, M, Ls, tag);
    }
    STAMP(7);
    wg_barrier();
    STAMP(8);
    {
      const int kind = tid >> 7, col = tid & 127;
      if (col < 80 && (kind == 0 || ffn)) {
        const float* src = reinterpret_cast<const float*>(kind ? &L.df[0][0] : &L.ds[0][0]) + ((col >> 5) * 16 + (col & 7)) * 4 + ((col >> 3) & 3);
        float v0 = 0.f, v1 = 0.f;
#pragma unroll
        for (int a = 0; a < 4; ++a) { v0 += src[a * 256]; v1 += src[a * 256 + 32]; }
        st_unit(rws, (kind ? WS_FP + (i * 32 + gi) * 1280 : WS_SP + (i * 32 + h) * 1280) + col * 16, v0, v1, tag);
      }
    }
    STAMP(9);
    STAMP_FLUSH();
  } else {
    __builtin_amdgcn_s_setprio(3);
    unsigned selE[4], selO[4];
#pragma unroll
    for (int jj = 0; jj < 4; ++jj) { selE[jj] = psel(n & 7, 0, jj, true); selO[jj] = psel(n & 7, 1, jj, true); }
    if (st1) {
      const unsigned tag = ep + 1u;
      ln_slice(xr, lane, i, &L.xn[a][0][0]);
      v2u xw[5];
#pragma unroll
      for (int u = 0; u < 5; ++u) xw[u] = *reinterpret_cast<const v2u*>(&L.xn[a][n >> 3][20 * g + 4 * u]);
      STAMP(1);
      f32x4 dq = {0.f, 0.f, 0.f, 0.f};
      if (a == 0) {
#pragma unroll
        for (int t = 0; t < 20; ++t) { const unsigned src = (t & 2) ? xw[t >> 2].y : xw[t >> 2].x; dq = mfma32(W[t], perm4(src, (t & 1) ? selO : selE), dq); }
      } else {
        f32x4 d1 = {0.f, 0.f, 0.f, 0.f};
#pragma unroll
        for (int u = 0; u < 10; ++u) { const unsigned src = (u & 1) ? xw[u >> 1].y : xw[u >> 1].x; dq = mfma32(W[u], perm4(src, selE), dq); d1 = mfma32(W[u], perm4(src, selO), d1); }
#pragma unroll
        for (int k = 0; k < 4; ++k) dq[k] = x32_sum(g < 2 ? dq[k] : d1[k]);
      }
      const int po = a ? WS_HP + (gi * 8 + i) * 1024 + (n >> 3) * 512 : WS_QP + ((h * 8 + i) * 2 + (n >> 3)) * 1024;
      if (!a || g < 2) { const int qo = po + (g * 8 + (n & 7)) * 32; st_unit(rws, qo, dq[0], dq[1], tag); st_unit(rws, qo + 16, dq[2], dq[3], tag); }
      STAMP(2);
      __builtin_amdgcn_s_sleep(GATHER_SLEEP);
      const int go = (a ? WS_HP + gi * 8192 : WS_QP + (h * 16 + b) * 1024) + lane * 16, gs = a ? 1024 : 2048;
      v4u G[8];
      for (unsigned spin = 0;; ++spin) {
        MEMBAR();
#pragma unroll
        for (int p = 0; p < 8; ++p) G[p] = ld_sc1(rws, go, p * gs);
        bool ok = true;
#pragma unroll
        for (int p = 0; p < 8; ++p) ok &= unit_ok(G[p], tag);
        NPOLL(0);
        if (__all(ok) || spin > SPIN_CAP) break;
        __builtin_amdgcn_s_sleep(8);
      }
      float q0 = 0.f, q1 = 0.f;
#pragma unroll
      for (int p = 0; p < 8; ++p) { q0 += ux(G[p]); q1 += uz(G[p]); }
      if (a) { q0 = fmaxf(q0, 0.f); q1 = fmaxf(q1, 0.f); }
      const unsigned qq = cvtpk(q0, q1);
      const int d0 = 32 * g + 16 * (lane & 1) + ((lane >> 1) & 7);
      L.qh[a][d0] = (unsigned short)qq; L.qh[a][d0 + 8] = (unsigned short)(qq >> 16);
      if (tid == 256) L.tag = tag;
      STAMP(3);
    }
    wg_barrier();
    const unsigned tag = L.tag;
    const v2u hw = *reinterpret_cast<const v2u*>(&L.qh[1][(n >> 3) * 64 + 16 * a + 4 * g]);
    unsigned xres = 0;
    if (a == 0 && h < 20 && lane < 8) xres = x[(lane >> 2) * D + i * 80 + h * 4 + (lane & 3)];
    wg_barrier();
    STAMP(4);
    const rsrc_t r2 = mkrsrc(w2, DFF * D * 2), ro = mkrsrc(wo, H * DH * D * 2);
    v4u W2[4], WO[8];
    {
      const int v2o = (n < 10 && ffn) ? (64 * gi + 16 * a + 4 * g) * 1280 + i * 160 + n * 16 : OOB;
      const int voo = n < 10 ? (128 * h + 32 * a + 2 * g) * 1280 + i * 160 + n * 16 : OOB;
#pragma unroll
      for (int t = 0; t < 4; ++t) W2[t] = ld_nt(r2, v2o, t * 1280);
#pragma unroll
      for (int t = 0; t < 8; ++t) WO[t] = ld_nt(ro, voo, (8 * (t & 3) + (t >> 2)) * 1280);
      SB();
    }
    wg_barrier();
    f32x4 df = {0.f, 0.f, 0.f, 0.f};
    df = mfma32(W2[0], perm4(hw.x, selE), df); df = mfma32(W2[1], perm4(hw.x, selO), df);
    df = mfma32(W2[2], perm4(hw.y, selE), df); df = mfma32(W2[3], perm4(hw.y, selO), df);
    L.df[a][lane] = df;
    STAMP(5);
    v4u A0, A1, AM;
    asm volatile("" :: "v"(WO[7]));
    {
      const int po = WS_AP + (h * 8 + (n >> 3) * 4 + (n & 3)) * 1280;
      const int uo = po + (16 * a + 4 * g + 2 * ((n >> 2) & 1)) * 16;
      for (unsigned spin = 0;; ++spin) {
        MEMBAR();
        A0 = ld_sc1(rws, uo, 0); A1 = ld_sc1(rws, uo, 16); AM = ld_sc1(rws, po, 1024);
        NPOLL(1);
        if (__all(unit_ok(A0, tag) & unit_ok(A1, tag) & unit_ok(AM, tag)) || spin > SPIN_CAP) break;
        __builtin_amdgcn_s_sleep(2);
      }
    }
    STAMP(6);
    {
      float M = ux(AM); M = fmaxf(M, dppf<XOR1>(M)); M = fmaxf(M, dppf<XOR2>(M));
      const float w = __builtin_amdgcn_exp2f((ux(AM) - M) * 1.4426950408889634f);
      float Ls = w * uz(AM); Ls += dppf<XOR1>(Ls); Ls += dppf<XOR2>(Ls);
      const float wn = w / Ls;
      float o0 = wn * ux(A0), o1 = wn * uz(A0), o2 = wn * ux(A1), o3 = wn * uz(A1);
      o0 += dppf<XOR1>(o0); o1 += dppf<XOR1>(o1); o2 += dppf<XOR1>(o2); o3 += dppf<XOR1>(o3);
      o0 += dppf<XOR2>(o0); o1 += dppf<XOR2>(o1); o2 += dppf<XOR2>(o2); o3 += dppf<XOR2>(o3);
      const unsigned m01 = cvtpk(o0, o1), m23 = cvtpk(o2, o3), t01 = dppu<HMIR>(m01), t23 = dppu<HMIR>(m23);
      const bool hf = (n & 4) != 0;
      const unsigned ow[4] = {hf ? t01 : m01, hf ? t23 : m23, hf ? m01 : t01, hf ? m23 : t23};
      f32x4 ds = {0.f, 0.f, 0.f, 0.f};
#pragma unroll
      for (int t = 0; t < 8; ++t) ds = mfma32(WO[t], perm4(ow[t >> 1], (t & 1) ? selO : selE), ds);
      L.ds[a][lane] = ds;
    }
    STAMP(7);
    wg_barrier();
    if (a == 0 && h < 20) {
      const int p = lane >> 2, co = (h * 4 + (lane & 3)) * 16;
      const int so = WS_SP + (i * 32 + p) * 1280 + co, fo = WS_FP + (i * 32 + p) * 1280 + co;
      const bool f2 = p + 16 < NG;
      v4u S0, S1, F0, F1;
      for (unsigned spin = 0;; ++spin) {
        MEMBAR();
        S0 = ld_sc1(rws, so, 0); S1 = ld_sc1(rws, so, 16 * 1280); F0 = ld_sc1(rws, fo, 0); F1 = ld_sc1(rws, f2 ? fo : OOB, 16 * 1280);
        NPOLL(2);
        if (__all(unit_ok(S0, tag) & unit_ok(S1, tag) & unit_ok(F0, tag) & (!f2 | unit_ok(F1, tag))) || spin > SPIN_CAP) break;
        __builtin_amdgcn_s_sleep(2);
      }
      STAMP(8);
      float r[4] = {ux(S0) + ux(S1), uz(S0) + uz(S1), ux(F0) + ux(F1), uz(F0) + uz(F1)};
#pragma unroll
      for (int k = 0; k < 4; ++k) { float t = r[k]; t += dppf<ROR8>(t); t += dppf<ROR4>(t); r[k] = x32_sum(x16_sum(t)); }
      const float sv = rbf((lane & 4) ? r[1] : r[0]), fv = rbf((lane & 4) ? r[3] : r[2]);
      const float gv = rbf(fv / (1.0f + __expf(-fv)));
      const float o = bflo(xres) + rbf(sv * gv);
      if (lane < 8) out[(lane >> 2) * D + i * 80 + h * 4 + (lane & 3)] = (unsigned short)cvtpk(o, o);
      if (bid == 0 && lane == 0) __builtin_amdgcn_raw_buffer_store_b32(tag, rws, WS_EPOCH, 0, AUX_SC1);
      STAMP(9);
    }
    STAMP_FLUSH();
  }
}
}

extern "C" void kernel_launch(void* const* d_in, const int* in_sizes, int n_in, void* d_out, int out_size, void* d_ws, size_t ws_size, hipStream_t stream) {
  fused_decode<<<256, 512, 0, stream>>>((const unsigned short*)d_in[0], (const unsigned short*)d_in[1], (const unsigned short*)d_in[2],
                                        (const unsigned short*)d_in[3], (const unsigned short*)d_in[4], (const unsigned short*)d_in[6],
                                        (const unsigned short*)d_in[7], (unsigned short*)d_out, (unsigned char*)d_ws);
}
```

```cpp
#define PROBE_W 4
#define PROBE_K 15
#define PROBE_B 0
#define PFENCE 1
#include <hip/hip_runtime.h>
#include <hip/hip_bf16.h>
#include <stdint.h>

namespace {
typedef unsigned v4u __attribute__((ext_vector_type(4)));
typedef unsigned v2u __attribute__((ext_vector_type(2)));
typedef float f32x4 __attribute__((ext_vector_type(4)));
typedef float f32x2 __attribute__((ext_vector_type(2)));
typedef short s16x4 __attribute__((ext_vector_type(4)));
typedef __bf16 bf16x2 __attribute__((ext_vector_type(2)));
typedef __bf16 bf16x8 __attribute__((ext_vector_type(8)));
typedef __amdgpu_buffer_rsrc_t rsrc_t;

constexpr int D = 640, H = 32, SK = 8192, DH = 128, WIN = 2048, DFF = 1728, NG = 27;
constexpr int AUX_NT = 2, AUX_SC1 = 16, RSRC3 = 0x00020000;
constexpr int OOB = 0x7ffffff0;
constexpr int WS_EPOCH = 0;
constexpr int WS_QP = 4096;
constexpr int WS_HP = WS_QP + 32 * 8 * 2 * 1024;
constexpr int WS_AP = WS_HP + 32 * 8 * 1024;
constexpr int WS_SP = WS_AP + 32 * 8 * 1280;
constexpr int WS_FP = WS_SP + 8 * 32 * 1280;
constexpr int WS_END = WS_FP + 8 * 32 * 1280;
constexpr unsigned SPIN_CAP = 1u << 18;
#define GATHER_SLEEP 40
#define XAUX 0
#define KWIN 12
#define KPRE 2
#define KDELAY 10
#define CODE_LINES 104
#define VPRE 16
#define VRING 20
#define STAMP(k) do { SB(); st_[k] = (unsigned)__builtin_amdgcn_s_memrealtime(); SB(); } while (0)
#define STAMP_FLUSH() do { } while (0)
#define NPOLL(k) do { } while (0)

#define SB() __builtin_amdgcn_sched_barrier(0)
#define MEMBAR() asm volatile("" ::: "memory")
#define VMCNT(n) asm volatile("s_waitcnt vmcnt(%0)" ::"n"(n) : "memory")
__device__ __forceinline__ void wg_barrier() { SB(); asm volatile("s_waitcnt lgkmcnt(0)\n\ts_barrier" ::: "memory"); SB(); }
__device__ __forceinline__ float bflo(unsigned u) { return __uint_as_float(u << 16); }
__device__ __forceinline__ float bfhi(unsigned u) { return __uint_as_float(u & 0xffff0000u); }
__device__ __forceinline__ unsigned cvtpk(float lo, float hi) { bf16x2 v = {(__bf16)lo, (__bf16)hi}; return __builtin_bit_cast(unsigned, v); }
__device__ __forceinline__ float rbf(float f) { return __uint_as_float(cvtpk(f, f) & 0xffff0000u); }
template <int CTRL> __device__ __forceinline__ float dppf(float x) {
  return __builtin_bit_cast(float, __builtin_amdgcn_mov_dpp(__builtin_bit_cast(int, x), CTRL, 0xf, 0xf, true));
}
template <int CTRL> __device__ __forceinline__ unsigned dppu(unsigned x) { return (unsigned)__builtin_amdgcn_mov_dpp((int)x, CTRL, 0xf, 0xf, true); }
constexpr int XOR1 = 0xB1, XOR2 = 0x4E, HMIR = 0x141, ROR8 = 0x128, ROR4 = 0x124, NEWB = 0x150;
__device__ __forceinline__ float x16_sum(float x) { auto s = __builtin_amdgcn_permlane16_swap(__float_as_uint(x), __float_as_uint(x), false, false); return __uint_as_float(s[0]) + __uint_as_float(s[1]); }
__device__ __forceinline__ float x32_sum(float x) { auto s = __builtin_amdgcn_permlane32_swap(__float_as_uint(x), __float_as_uint(x), false, false); return __uint_as_float(s[0]) + __uint_as_float(s[1]); }
__device__ __forceinline__ float x16_max(float x) { auto s = __builtin_amdgcn_permlane16_swap(__float_as_uint(x), __float_as_uint(x), false, false); return fmaxf(__uint_as_float(s[0]), __uint_as_float(s[1])); }
__device__ __forceinline__ float x32_max(float x) { auto s = __builtin_amdgcn_permlane32_swap(__float_as_uint(x), __float_as_uint(x), false, false); return fmaxf(__uint_as_float(s[0]), __uint_as_float(s[1])); }
__device__ __forceinline__ float row_sum(float x) { x += dppf<XOR1>(x); x += dppf<XOR2>(x); x += dppf<HMIR>(x); x += dppf<ROR8>(x); return x; }
__device__ __forceinline__ float row_max(float x) { x = fmaxf(x, dppf<XOR1>(x)); x = fmaxf(x, dppf<XOR2>(x)); x = fmaxf(x, dppf<HMIR>(x)); x = fmaxf(x, dppf<ROR8>(x)); return x; }
__device__ __forceinline__ float dot2(unsigned k, unsigned q, float acc) {
  return __builtin_amdgcn_fdot2_f32_bf16(__builtin_bit_cast(bf16x2, k), __builtin_bit_cast(bf16x2, q), acc, false);
}
__device__ __forceinline__ f32x4 mfma32(v4u a, v4u b, f32x4 c) {
  return __builtin_amdgcn_mfma_f32_16x16x32_bf16(__builtin_bit_cast(bf16x8, a), __builtin_bit_cast(bf16x8, b), c, 0, 0, 0);
}
__device__ __forceinline__ f32x4 mfma16(v2u a, v2u b, f32x4 c) {
  return __builtin_amdgcn_mfma_f32_16x16x16bf16_1k(__builtin_bit_cast(s16x4, a), __builtin_bit_cast(s16x4, b), c, 0, 0, 0);
}
__device__ __forceinline__ rsrc_t mkrsrc(const void* p, unsigned bytes) { return __builtin_amdgcn_make_buffer_rsrc((void*)p, 0, bytes, RSRC3); }
__device__ __forceinline__ v4u ld_nt(rsrc_t r, int voff, int soff) { return __builtin_amdgcn_raw_buffer_load_b128(r, voff, soff, AUX_NT); }
__device__ __forceinline__ v2u ld_nt64(rsrc_t r, int voff, int soff) { return __builtin_amdgcn_raw_buffer_load_b64(r, voff, soff, AUX_NT); }
__device__ __forceinline__ v4u ld_sc1(rsrc_t r, int voff, int soff) { return __builtin_amdgcn_raw_buffer_load_b128(r, voff, soff, AUX_SC1); }
__device__ __forceinline__ void st_unit(rsrc_t r, int voff, float a, float b, unsigned tag) {
  v4u v = {__float_as_uint(a), tag, __float_as_uint(b), tag};
  __builtin_amdgcn_raw_buffer_store_b128(v, r, voff, 0, AUX_SC1);
}
__device__ __forceinline__ bool unit_ok(v4u g, unsigned tag) { return (g.y == tag) & (g.w == tag); }
__device__ __forceinline__ float ux(v4u g) { return __uint_as_float(g.x); }
__device__ __forceinline__ float uz(v4u g) { return __uint_as_float(g.z); }
__device__ __forceinline__ unsigned psel(int e, int sh, int jj, bool on) {
  const unsigned pair = (unsigned)(2 * sh) | ((unsigned)(2 * sh + 1) << 8);
  const unsigned s = (e & 1) ? ((pair << 16) | 0x0c0cu) : (0x0c0c0000u | pair);
  return (on && (e >> 1) == jj) ? s : 0x0c0c0c0cu;
}
__device__ __forceinline__ unsigned perm(unsigned src, unsigned sel) { return __builtin_amdgcn_perm(src, src, sel); }
__device__ __forceinline__ v4u perm4(unsigned src, const unsigned (&s)[4]) { return v4u{perm(src, s[0]), perm(src, s[1]), perm(src, s[2]), perm(src, s[3])}; }

struct __attribute__((aligned(16))) Lds {
  f32x4 ds[4][64];
  f32x4 df[4][64];
  float acc[4][128];
  float ml[8];
  unsigned short xn[2][2][80];
  unsigned short qh[2][128];
  unsigned tag; unsigned pad[3];
  unsigned probe;
};

struct XRegs { v4u c[3]; };
__device__ __forceinline__ void x_issue(XRegs& xr, rsrc_t rx, int lane) {
  const int vo = (lane >> 5) * 1280 + (lane & 31) * 16;
  xr.c[0] = __builtin_amdgcn_raw_buffer_load_b128(rx, vo, 0, XAUX);
  xr.c[1] = __builtin_amdgcn_raw_buffer_load_b128(rx, vo, 512, XAUX);
  xr.c[2] = __builtin_amdgcn_raw_buffer_load_b128(rx, (lane & 16) ? OOB : vo, 1024, XAUX);
}
__device__ __forceinline__ void ln_slice(const XRegs& xr, int lane, int i, unsigned short* dst) {
  float s = 0.f;
#pragma unroll
  for (int t = 0; t < 3; ++t)
#pragma unroll
    for (int k = 0; k < 4; ++k) s += bflo(xr.c[t][k]) + bfhi(xr.c[t][k]);
  s = x16_sum(row_sum(s));
  const float mean = s * (1.0f / D);
  float ss = 0.f, s2 = 0.f;
#pragma unroll
  for (int t = 0; t < 2; ++t)
#pragma unroll
    for (int k = 0; k < 4; ++k) { const float d0 = bflo(xr.c[t][k]) - mean, d1 = bfhi(xr.c[t][k]) - mean; ss = fmaf(d0, d0, ss); ss = fmaf(d1, d1, ss); }
#pragma unroll
  for (int k = 0; k < 4; ++k) { const float d0 = bflo(xr.c[2][k]) - mean, d1 = bfhi(xr.c[2][k]) - mean; s2 = fmaf(d0, d0, s2); s2 = fmaf(d1, d1, s2); }
  ss += (lane & 16) ? 0.f : s2;
  ss = x16_sum(row_sum(ss));
  const float mb = rbf(mean), den = rbf(sqrtf(rbf(rbf(ss * (1.0f / D)) + 1e-5f)));
  const int rel = ((lane & 31) - 10 * i) & 31;
  const int cidx = 10 * i + rel;
  const v4u m = cidx < 32 ? xr.c[0] : cidx < 64 ? xr.c[1] : xr.c[2];
  v4u o;
#pragma unroll
  for (int k = 0; k < 4; ++k) o[k] = cvtpk(rbf(bflo(m[k]) - mb) / den, rbf(bfhi(m[k]) - mb) / den);
  if (rel < 10) *reinterpret_cast<v4u*>(dst + (lane >> 5) * 80 + rel * 8) = o;
  asm volatile("s_waitcnt lgkmcnt(0)" ::: "memory");
}

__global__ void __launch_bounds__(512, 2) __attribute__((aligned(4096)))
fused_decode(const unsigned short* __restrict__ x, const unsigned short* __restrict__ kc, const unsigned short* __restrict__ vc,
             const unsigned short* __restrict__ wq, const unsigned short* __restrict__ wo, const unsigned short* __restrict__ w1,
             const unsigned short* __restrict__ w2, unsigned short* __restrict__ out, unsigned char* __restrict__ ws) {
  __shared__ Lds L;
  const int tid = threadIdx.x, lane = tid & 63, wave = __builtin_amdgcn_readfirstlane(tid >> 6);
  const int bid = blockIdx.x, xcd = bid & 7, sl = bid >> 3, kq = sl >> 3, h = xcd * 4 + kq, i = sl & 7, b = i >> 2, j = i & 3;
  const int gi = kq * 8 + xcd;
  const bool ffn = gi < NG;
  const int g = lane >> 4, n = lane & 15;
  const rsrc_t rws = mkrsrc(ws, WS_END);
  unsigned st_[16] = {0, 0, 0, 0, 0, 0, 0, 0, 0, 0, 0, 0, 0, 0, 0, 0};
  st_[15] = (unsigned)__builtin_amdgcn_s_memrealtime(); SB();
  asm volatile("" :: "s"(x), "s"(kc), "s"(vc), "s"(wq), "s"(wo), "s"(w1), "s"(w2), "s"(out), "s"(ws));
  STAMP(0);
  const size_t kvbase = ((size_t)(b * H + h) * SK + (SK - WIN) + j * 512) * DH;
  const rsrc_t rk = mkrsrc(kc + kvbase, 131072);
  const int lo = wave * 1024 + lane * 16;
  v4u K[32];
  const int a = wave - 4;
  const bool st1 = a == 0 || (a == 1 && ffn);
  unsigned ep = 0; XRegs xr; v4u W[20];
  if (st1) {
    __builtin_amdgcn_s_setprio(3);
    const rsrc_t rx = mkrsrc(x, 2 * D * 2);
    STAMP(11);
    x_issue(xr, rx, lane);
    ep = __builtin_amdgcn_raw_buffer_load_b32(rws, WS_EPOCH, 0, AUX_SC1);
    STAMP(12);
    if (a == 0) {
      const rsrc_t rw = mkrsrc(wq, D * H * DH * 2);
      const int vo = (80 * i + 20 * g) * 8192 + h * 256 + n * 16;
#pragma unroll
      for (int t = 0; t < 20; ++t) W[t] = ld_nt(rw, vo, t * 8192);
    } else {
      const rsrc_t rw = mkrsrc(w1, D * DFF * 2);
      const int vo = (80 * i + 20 * g + (n >> 3)) * (DFF * 2) + gi * 128 + (n & 7) * 16;
#pragma unroll
      for (int u = 0; u < 10; ++u) W[u] = ld_nt(rw, vo, u * (DFF * 4));
    }
    SB();
    STAMP(10);
  } else if (wave < 4) {
#pragma unroll
    for (int t = 0; t < KPRE; ++t) K[t] = ld_nt(rk, lo, t * 4096);
  }
  else if (a >= 2) {
    unsigned long long pc;
    asm volatile("s_getpc_b64 %0" : "=s"(pc));
    pc &= ~4095ull;
    const int l0 = sl * 2 + (a - 2), l1 = l0 + 64 < CODE_LINES ? l0 + 64 : l0;
    const unsigned long long p0 = pc + (unsigned)(CODE_LINES - 1 - l0) * 128u, p1 = pc + (unsigned)(CODE_LINES - 1 - l1) * 128u;
    unsigned d0, d1;
    asm volatile("s_load_dword %0, %2, 0x0\n\ts_load_dword %1, %3, 0x0\n\ts_waitcnt lgkmcnt(0)" : "=&s"(d0), "=&s"(d1) : "s"(p0), "s"(p1));
  }
  int wave2 = wave;
  asm volatile("" : "+s"(wave2));
  SB();
  if (__builtin_expect(wave2 < 4, 1)) {
    const rsrc_t rv = mkrsrc(vc + kvbase, 131072);
    v4u V[32];
    STAMP(10);
    __builtin_amdgcn_s_sleep(KDELAY);
    STAMP(11);
#pragma unroll
    for (int t = KPRE; t < 32; ++t) { if (t >= KWIN) VMCNT(KWIN - 1); if (t == KWIN) STAMP(12); if (t == 31) STAMP(13); K[t] = ld_nt(rk, lo, t * 4096); SB(); }
#pragma unroll
    for (int t = 0; t < VPRE; ++t) { VMCNT(KWIN - 1); V[t] = ld_nt(rv, lo, t * 4096); SB(); }
    unsigned sel[4];
#pragma unroll
    for (int jj = 0; jj < 4; ++jj) sel[jj] = psel(n & 7, n >> 3, jj, true);
    STAMP(1);
    wg_barrier();
    STAMP(2);
#pragma unroll
    for (int t = VPRE; t < VRING; ++t) { V[t] = ld_nt(rv, lo, t * 4096); SB(); }
    const v4u qv = *reinterpret_cast<const v4u*>(&L.qh[0][n * 8]);
    const unsigned tag = L.tag;
    float xs[32];
#pragma unroll
    for (int t = 0; t < 32; ++t) { float d = dot2(K[t].x, qv.x, 0.f); d = dot2(K[t].y, qv.y, d); d = dot2(K[t].z, qv.z, d); xs[t] = dot2(K[t].w, qv.w, d); }
#define TR_STEP(HALF, CTRL, BIT) _Pragma("unroll") for (int u = 0; u < (HALF); ++u) { \
      const float lo_ = xs[u], hi_ = xs[u + (HALF)]; const float t1_ = lo_ + dppf<CTRL>(lo_), t2_ = hi_ + dppf<CTRL>(hi_); xs[u] = (n & (BIT)) ? t2_ : t1_; }
    TR_STEP(16, ROR8, 8) TR_STEP(8, HMIR, 4) TR_STEP(4, XOR2, 2) TR_STEP(2, XOR1, 1)
#undef TR_STEP
    const float s0 = rbf(rbf(xs[0]) * 0.08838834764831845f), s1 = rbf(rbf(xs[1]) * 0.08838834764831845f);
    const float m = x32_max(x16_max(row_max(fmaxf(s0, s1))));
    const float p0 = __builtin_amdgcn_exp2f((s0 - m) * 1.4426950408889634f), p1 = __builtin_amdgcn_exp2f((s1 - m) * 1.4426950408889634f);
    const unsigned P0 = cvtpk(p0, p0 - rbf(p0)), P1 = cvtpk(p1, p1 - rbf(p1));
    const float lsum = x32_sum(x16_sum(row_sum(p0 + p1)));
    STAMP(3);
    f32x4 acc = {0.f, 0.f, 0.f, 0.f};
#define PV(t) { const unsigned bc = dppu<NEWB + ((t) >> 1)>(((t) & 1) ? P1 : P0); acc = mfma32(V[t], perm4(bc, sel), acc); \
      if ((t) + VRING < 32) V[((t) + VRING) & 31] = ld_nt(rv, lo, ((t) + VRING) * 4096); SB(); \
      if ((t) + VRING == 31) { STAMP(4); wg_barrier(); } }
    PV(0) PV(1) PV(2) PV(3) PV(4) PV(5) PV(6) PV(7) PV(8) PV(9) PV(10) PV(11) PV(12) PV(13) PV(14) PV(15)
    PV(16) PV(17) PV(18) PV(19) PV(20) PV(21) PV(22) PV(23) PV(24) PV(25) PV(26) PV(27) PV(28) PV(29) PV(30) PV(31)
#undef PV
#pragma unroll
    for (int k = 0; k < 4; ++k) acc[k] += dppf<ROR8>(acc[k]);
    if (n < 8) *reinterpret_cast<f32x4*>(&L.acc[wave][(g * 8 + n) * 4]) = acc;
    if (lane == 0) { L.ml[wave] = m; L.ml[4 + wave] = lsum; }
    STAMP(5);
    wg_barrier();
    STAMP(6);
    if (wave == 0) {
      const f32x4 mm = *reinterpret_cast<const f32x4*>(&L.ml[0]), ll = *reinterpret_cast<const f32x4*>(&L.ml[4]);
      f32x2 a[4];
#pragma unroll
      for (int w = 0; w < 4; ++w) a[w] = *reinterpret_cast<const f32x2*>(&L.acc[w][2 * lane]);
      const float M = fmaxf(fmaxf(mm[0], mm[1]), fmaxf(mm[2], mm[3]));
      float v0 = 0.f, v1 = 0.f, Ls = 0.f;
#pragma unroll
      for (int w = 0; w < 4; ++w) { const float f = __builtin_amdgcn_exp2f((mm[w] - M) * 1.4426950408889634f); v0 = fmaf(f, a[w][0], v0); v1 = fmaf(f, a[w][1], v1); Ls = fmaf(f, ll[w], Ls); }
      const int ap = WS_AP + (h * 8 + i) * 1280;
      st_unit(rws, ap + lane * 16, v0, v1, tag);
      if (lane == 0) st_unit(rws, ap + 1024, M, Ls, tag);
    }
    STAMP(7);
    if (wave == PROBE_W && lane == 0) L.probe = st_[PROBE_K] - st_[15];
    wg_barrier();
    STAMP(8);
    {
      const int kind = tid >> 7, col = tid & 127;
      if (col < 80 && (kind == 0 || ffn)) {
        const float* src = reinterpret_cast<const float*>(kind ? &L.df[0][0] : &L.ds[0][0]) + ((col >> 5) * 16 + (col & 7)) * 4 + ((col >> 3) & 3);
        float v0 = 0.f, v1 = 0.f;
#pragma unroll
        for (int a = 0; a < 4; ++a) { v0 += src[a * 256]; v1 += src[a * 256 + 32]; }
        st_unit(rws, (kind ? WS_FP + (i * 32 + gi) * 1280 : WS_SP + (i * 32 + h) * 1280) + col * 16, v0, v1, tag);
      }
    }
    STAMP(9);
    STAMP_FLUSH();
  } else {
    __builtin_amdgcn_s_setprio(3);
    unsigned selE[4], selO[4];
#pragma unroll
    for (int jj = 0; jj < 4; ++jj) { selE[jj] = psel(n & 7, 0, jj, true); selO[jj] = psel(n & 7, 1, jj, true); }
    if (st1) {
      const unsigned tag = ep + 1u;
      asm volatile("" :: "v"(xr.c[0]), "v"(xr.c[2])); STAMP(13); asm volatile("" :: "v"(ep)); STAMP(14);
      ln_slice(xr, lane, i, &L.xn[a][0][0]);
      v2u xw[5];
#pragma unroll
      for (int u = 0; u < 5; ++u) xw[u] = *reinterpret_cast<const v2u*>(&L.xn[a][n >> 3][20 * g + 4 * u]);
      STAMP(1);
      f32x4 dq = {0.f, 0.f, 0.f, 0.f};
      if (a == 0) {
#pragma unroll
        for (int t = 0; t < 20; ++t) { const unsigned src = (t & 2) ? xw[t >> 2].y : xw[t >> 2].x; dq = mfma32(W[t], perm4(src, (t & 1) ? selO : selE), dq); }
      } else {
        f32x4 d1 = {0.f, 0.f, 0.f, 0.f};
#pragma unroll
        for (int u = 0; u < 10; ++u) { const unsigned src = (u & 1) ? xw[u >> 1].y : xw[u >> 1].x; dq = mfma32(W[u], perm4(src, selE), dq); d1 = mfma32(W[u], perm4(src, selO), d1); }
#pragma unroll
        for (int k = 0; k < 4; ++k) dq[k] = x32_sum(g < 2 ? dq[k] : d1[k]);
      }
      const int po = a ? WS_HP + (gi * 8 + i) * 1024 + (n >> 3) * 512 : WS_QP + ((h * 8 + i) * 2 + (n >> 3)) * 1024;
      if (!a || g < 2) { const int qo = po + (g * 8 + (n & 7)) * 32; st_unit(rws, qo, dq[0], dq[1], tag); st_unit(rws, qo + 16, dq[2], dq[3], tag); }
      STAMP(2);
      __builtin_amdgcn_s_sleep(GATHER_SLEEP);
      const int go = (a ? WS_HP + gi * 8192 : WS_QP + (h * 16 + b) * 1024) + lane * 16, gs = a ? 1024 : 2048;
      v4u G[8];
      for (unsigned spin = 0;; ++spin) {
        MEMBAR();
#pragma unroll
        for (int p = 0; p < 8; ++p) G[p] = ld_sc1(rws, go, p * gs);
        bool ok = true;
#pragma unroll
        for (int p = 0; p < 8; ++p) ok &= unit_ok(G[p], tag);
        NPOLL(0);
        if (__all(ok) || spin > SPIN_CAP) break;
        __builtin_amdgcn_s_sleep(8);
      }
      float q0 = 0.f, q1 = 0.f;
#pragma unroll
      for (int p = 0; p < 8; ++p) { q0 += ux(G[p]); q1 += uz(G[p]); }
      if (a) { q0 = fmaxf(q0, 0.f); q1 = fmaxf(q1, 0.f); }
      const unsigned qq = cvtpk(q0, q1);
      const int d0 = 32 * g + 16 * (lane & 1) + ((lane >> 1) & 7);
      L.qh[a][d0] = (unsigned short)qq; L.qh[a][d0 + 8] = (unsigned short)(qq >> 16);
      if (tid == 256) L.tag = tag;
      STAMP(3);
    }
    wg_barrier();
    const unsigned tag = L.tag;
    const v2u hw = *reinterpret_cast<const v2u*>(&L.qh[1][(n >> 3) * 64 + 16 * a + 4 * g]);
    unsigned xres = 0;
    if (a == 0 && h < 20 && lane < 8) xres = x[(lane >> 2) * D + i * 80 + h * 4 + (lane & 3)];
    wg_barrier();
    STAMP(4);
    const rsrc_t r2 = mkrsrc(w2, DFF * D * 2), ro = mkrsrc(wo, H * DH * D * 2);
    v4u W2[4], WO[8];
    {
      const int v2o = (n < 10 && ffn) ? (64 * gi + 16 * a + 4 * g) * 1280 + i * 160 + n * 16 : OOB;
      const int voo = n < 10 ? (128 * h + 32 * a + 2 * g) * 1280 + i * 160 + n * 16 : OOB;
#pragma unroll
      for (int t = 0; t < 4; ++t) W2[t] = ld_nt(r2, v2o, t * 1280);
#pragma unroll
      for (int t = 0; t < 8; ++t) WO[t] = ld_nt(ro, voo, (8 * (t & 3) + (t >> 2)) * 1280);
      SB();
    }
    wg_barrier();
    f32x4 df = {0.f, 0.f, 0.f, 0.f};
    df = mfma32(W2[0], perm4(hw.x, selE), df); df = mfma32(W2[1], perm4(hw.x, selO), df);
    df = mfma32(W2[2], perm4(hw.y, selE), df); df = mfma32(W2[3], perm4(hw.y, selO), df);
    L.df[a][lane] = df;
    STAMP(5);
    v4u A0, A1, AM;
    asm volatile("" :: "v"(WO[7]));
    STAMP(12);
    {
      const int po = WS_AP + (h * 8 + (n >> 3) * 4 + (n & 3)) * 1280;
      const int uo = po + (16 * a + 4 * g + 2 * ((n >> 2) & 1)) * 16;
      for (unsigned spin = 0;; ++spin) {
        MEMBAR();
        A0 = ld_sc1(rws, uo, 0); A1 = ld_sc1(rws, uo, 16); AM = ld_sc1(rws, po, 1024);
        NPOLL(1);
        if (__all(unit_ok(A0, tag) & unit_ok(A1, tag) & unit_ok(AM, tag)) || spin > SPIN_CAP) break;
        __builtin_amdgcn_s_sleep(2);
      }
    }
    STAMP(6);
    {
      float M = ux(AM); M = fmaxf(M, dppf<XOR1>(M)); M = fmaxf(M, dppf<XOR2>(M));
      const float w = __builtin_amdgcn_exp2f((ux(AM) - M) * 1.4426950408889634f);
      float Ls = w * uz(AM); Ls += dppf<XOR1>(Ls); Ls += dppf<XOR2>(Ls);
      const float wn = w / Ls;
      float o0 = wn * ux(A0), o1 = wn * uz(A0), o2 = wn * ux(A1), o3 = wn * uz(A1);
      o0 += dppf<XOR1>(o0); o1 += dppf<XOR1>(o1); o2 += dppf<XOR1>(o2); o3 += dppf<XOR1>(o3);
      o0 += dppf<XOR2>(o0); o1 += dppf<XOR2>(o1); o2 += dppf<XOR2>(o2); o3 += dppf<XOR2>(o3);
      const unsigned m01 = cvtpk(o0, o1), m23 = cvtpk(o2, o3), t01 = dppu<HMIR>(m01), t23 = dppu<HMIR>(m23);
      const bool hf = (n & 4) != 0;
      const unsigned ow[4] = {hf ? t01 : m01, hf ? t23 : m23, hf ? m01 : t01, hf ? m23 : t23};
      f32x4 ds = {0.f, 0.f, 0.f, 0.f};
#pragma unroll
      for (int t = 0; t < 8; ++t) ds = mfma32(WO[t], perm4(ow[t >> 1], (t & 1) ? selO : selE), ds);
      L.ds[a][lane] = ds;
    }
    STAMP(7);
    wg_barrier();
    if (a == 3) { __builtin_amdgcn_fence(__ATOMIC_RELEASE, "agent"); asm volatile("s_waitcnt vmcnt(0)" ::: "memory"); }
    if (a == 0 && h < 20) {
      const int p = lane >> 2, co = (h * 4 + (lane & 3)) * 16;
      const int so = WS_SP + (i * 32 + p) * 1280 + co, fo = WS_FP + (i * 32 + p) * 1280 + co;
      const bool f2 = p + 16 < NG;
      v4u S0, S1, F0, F1;
      for (unsigned spin = 0;; ++spin) {
        MEMBAR();
        S0 = ld_sc1(rws, so, 0); S1 = ld_sc1(rws, so, 16 * 1280); F0 = ld_sc1(rws, fo, 0); F1 = ld_sc1(rws, f2 ? fo : OOB, 16 * 1280);
        NPOLL(2);
        if (__all(unit_ok(S0, tag) & unit_ok(S1, tag) & unit_ok(F0, tag) & (!f2 | unit_ok(F1, tag))) || spin > SPIN_CAP) break;
        __builtin_amdgcn_s_sleep(2);
      }
      STAMP(8);
      float r[4] = {ux(S0) + ux(S1), uz(S0) + uz(S1), ux(F0) + ux(F1), uz(F0) + uz(F1)};
#pragma unroll
      for (int k = 0; k < 4; ++k) { float t = r[k]; t += dppf<ROR8>(t); t += dppf<ROR4>(t); r[k] = x32_sum(x16_sum(t)); }
      const float sv = rbf((lane & 4) ? r[1] : r[0]), fv = rbf((lane & 4) ? r[3] : r[2]);
      const float gv = rbf(fv / (1.0f + __expf(-fv)));
      const float o = bflo(xres) + rbf(sv * gv);
      if (lane < 8) out[(lane >> 2) * D + i * 80 + h * 4 + (lane & 3)] = (unsigned short)cvtpk(o, o);
      if (bid == 0 && lane == 0) __builtin_amdgcn_raw_buffer_store_b32(tag, rws, WS_EPOCH, 0, AUX_SC1);
      STAMP(9);
      if (bid == PROBE_B) {
        const unsigned d = PROBE_W == 4 ? st_[PROBE_K] - st_[15] : L.probe;
        const unsigned until = st_[15] + 4000u + d;
        while ((int)((unsigned)__builtin_amdgcn_s_memrealtime() - until) < 0) __builtin_amdgcn_s_sleep(1);
      }
    }
    STAMP_FLUSH();
  }
}
}

extern "C" void kernel_launch(void* const* d_in, const int* in_sizes, int n_in, void* d_out, int out_size, void* d_ws, size_t ws_size, hipStream_t stream) {
  fused_decode<<<256, 512, 0, stream>>>((const unsigned short*)d_in[0], (const unsigned short*)d_in[1], (const unsigned short*)d_in[2],
                                        (const unsigned short*)d_in[3], (const unsigned short*)d_in[4], (const unsigned short*)d_in[6],
                                        (const unsigned short*)d_in[7], (unsigned short*)d_out, (unsigned char*)d_ws);
}
```

```cpp
#define PROBE_W 4
#define PROBE_K 15
#define PROBE_B 0
#define PFENCE 0
#define CODE_LINES 0
#include <hip/hip_runtime.h>
#include <hip/hip_bf16.h>
#include <stdint.h>

namespace {
typedef unsigned v4u __attribute__((ext_vector_type(4)));
typedef unsigned v2u __attribute__((ext_vector_type(2)));
typedef float f32x4 __attribute__((ext_vector_type(4)));
typedef float f32x2 __attribute__((ext_vector_type(2)));
typedef short s16x4 __attribute__((ext_vector_type(4)));
typedef __bf16 bf16x2 __attribute__((ext_vector_type(2)));
typedef __bf16 bf16x8 __attribute__((ext_vector_type(8)));
typedef __amdgpu_buffer_rsrc_t rsrc_t;

constexpr int D = 640, H = 32, SK = 8192, DH = 128, WIN = 2048, DFF = 1728, NG = 27;
constexpr int AUX_NT = 2, AUX_SC1 = 16, RSRC3 = 0x00020000;
constexpr int OOB = 0x7ffffff0;
constexpr int WS_EPOCH = 0;
constexpr int WS_QP = 4096;
constexpr int WS_HP = WS_QP + 32 * 8 * 2 * 1024;
constexpr int WS_AP = WS_HP + 32 * 8 * 1024;
constexpr int WS_SP = WS_AP + 32 * 8 * 1280;
constexpr int WS_FP = WS_SP + 8 * 32 * 1280;
constexpr int WS_END = WS_FP + 8 * 32 * 1280;
constexpr unsigned SPIN_CAP = 1u << 18;
#define GATHER_SLEEP 40
#define XAUX 0
#define KWIN 12
#define KPRE 2
#define KDELAY 10
#define VPRE 16
#define VRING 20
#define STAMP(k) do { SB(); st_[k] = (unsigned)__builtin_amdgcn_s_memrealtime(); SB(); } while (0)
#define STAMP_FLUSH() do { } while (0)
#define NPOLL(k) do { } while (0)

#define SB() __builtin_amdgcn_sched_barrier(0)
#define MEMBAR() asm volatile("" ::: "memory")
#define VMCNT(n) asm volatile("s_waitcnt vmcnt(%0)" ::"n"(n) : "memory")
__device__ __forceinline__ void wg_barrier() { SB(); asm volatile("s_waitcnt lgkmcnt(0)\n\ts_barrier" ::: "memory"); SB(); }
__device__ __forceinline__ float bflo(unsigned u) { return __uint_as_float(u << 16); }
__device__ __forceinline__ float bfhi(unsigned u) { return __uint_as_float(u & 0xffff0000u); }
__device__ __forceinline__ unsigned cvtpk(float lo, float hi) { bf16x2 v = {(__bf16)lo, (__bf16)hi}; return __builtin_bit_cast(unsigned, v); }
__device__ __forceinline__ float rbf(float f) { return __uint_as_float(cvtpk(f, f) & 0xffff0000u); }
template <int CTRL> __device__ __forceinline__ float dppf(float x) {
  return __builtin_bit_cast(float, __builtin_amdgcn_mov_dpp(__builtin_bit_cast(int, x), CTRL, 0xf, 0xf, true));
}
template <int CTRL> __device__ __forceinline__ unsigned dppu(unsigned x) { return (unsigned)__builtin_amdgcn_mov_dpp((int)x, CTRL, 0xf, 0xf, true); }
constexpr int XOR1 = 0xB1, XOR2 = 0x4E, HMIR = 0x141, ROR8 = 0x128, ROR4 = 0x124, NEWB = 0x150;
__device__ __forceinline__ float x16_sum(float x) { auto s = __builtin_amdgcn_permlane16_swap(__float_as_uint(x), __float_as_uint(x), false, false); return __uint_as_float(s[0]) + __uint_as_float(s[1]); }
__device__ __forceinline__ float x32_sum(float x) { auto s = __builtin_amdgcn_permlane32_swap(__float_as_uint(x), __float_as_uint(x), false, false); return __uint_as_float(s[0]) + __uint_as_float(s[1]); }
__device__ __forceinline__ float x16_max(float x) { auto s = __builtin_amdgcn_permlane16_swap(__float_as_uint(x), __float_as_uint(x), false, false); return fmaxf(__uint_as_float(s[0]), __uint_as_float(s[1])); }
__device__ __forceinline__ float x32_max(float x) { auto s = __builtin_amdgcn_permlane32_swap(__float_as_uint(x), __float_as_uint(x), false, false); return fmaxf(__uint_as_float(s[0]), __uint_as_float(s[1])); }
__device__ __forceinline__ float row_sum(float x) { x += dppf<XOR1>(x); x += dppf<XOR2>(x); x += dppf<HMIR>(x); x += dppf<ROR8>(x); return x; }
__device__ __forceinline__ float row_max(float x) { x = fmaxf(x, dppf<XOR1>(x)); x = fmaxf(x, dppf<XOR2>(x)); x = fmaxf(x, dppf<HMIR>(x)); x = fmaxf(x, dppf<ROR8>(x)); return x; }
__device__ __forceinline__ float dot2(unsigned k, unsigned q, float acc) {
  return __builtin_amdgcn_fdot2_f32_bf16(__builtin_bit_cast(bf16x2, k), __builtin_bit_cast(bf16x2, q), acc, false);
}
__device__ __forceinline__ f32x4 mfma32(v4u a, v4u b, f32x4 c) {
  return __builtin_amdgcn_mfma_f32_16x16x32_bf16(__builtin_bit_cast(bf16x8, a), __builtin_bit_cast(bf16x8, b), c, 0, 0, 0);
}
__device__ __forceinline__ f32x4 mfma16(v2u a, v2u b, f32x4 c) {
  return __builtin_amdgcn_mfma_f32_16x16x16bf16_1k(__builtin_bit_cast(s16x4, a), __builtin_bit_cast(s16x4, b), c, 0, 0, 0);
}
__device__ __forceinline__ rsrc_t mkrsrc(const void* p, unsigned bytes) { return __builtin_amdgcn_make_buffer_rsrc((void*)p, 0, bytes, RSRC3); }
__device__ __forceinline__ v4u ld_nt(rsrc_t r, int voff, int soff) { return __builtin_amdgcn_raw_buffer_load_b128(r, voff, soff, AUX_NT); }
__device__ __forceinline__ v2u ld_nt64(rsrc_t r, int voff, int soff) { return __builtin_amdgcn_raw_buffer_load_b64(r, voff, soff, AUX_NT); }
__device__ __forceinline__ v4u ld_sc1(rsrc_t r, int voff, int soff) { return __builtin_amdgcn_raw_buffer_load_b128(r, voff, soff, AUX_SC1); }
__device__ __forceinline__ void st_unit(rsrc_t r, int voff, float a, float b, unsigned tag) {
  v4u v = {__float_as_uint(a), tag, __float_as_uint(b), tag};
  __builtin_amdgcn_raw_buffer_store_b128(v, r, voff, 0, AUX_SC1);
}
__device__ __forceinline__ bool unit_ok(v4u g, unsigned tag) { return (g.y == tag) & (g.w == tag); }
__device__ __forceinline__ float ux(v4u g) { return __uint_as_float(g.x); }
__device__ __forceinline__ float uz(v4u g) { return __uint_as_float(g.z); }
__device__ __forceinline__ unsigned psel(int e, int sh, int jj, bool on) {
  const unsigned pair = (unsigned)(2 * sh) | ((unsigned)(2 * sh + 1) << 8);
  const unsigned s = (e & 1) ? ((pair << 16) | 0x0c0cu) : (0x0c0c0000u | pair);
  return (on && (e >> 1) == jj) ? s : 0x0c0c0c0cu;
}
__device__ __forceinline__ unsigned perm(unsigned src, unsigned sel) { return __builtin_amdgcn_perm(src, src, sel); }
__device__ __forceinline__ v4u perm4(unsigned src, const unsigned (&s)[4]) { return v4u{perm(src, s[0]), perm(src, s[1]), perm(src, s[2]), perm(src, s[3])}; }

struct __attribute__((aligned(16))) Lds {
  f32x4 ds[4][64];
  f32x4 df[4][64];
  float acc[4][128];
  float ml[8];
  unsigned short xn[2][2][80];
  unsigned short qh[2][128];
  unsigned tag; unsigned pad[3];
  unsigned probe;
};

struct XRegs { v4u c[3]; };
__device__ __forceinline__ void x_issue(XRegs& xr, rsrc_t rx, int lane) {
  const int vo = (lane >> 5) * 1280 + (lane & 31) * 16;
  xr.c[0] = __builtin_amdgcn_raw_buffer_load_b128(rx, vo, 0, XAUX);
  xr.c[1] = __builtin_amdgcn_raw_buffer_load_b128(rx, vo, 512, XAUX);
  xr.c[2] = __builtin_amdgcn_raw_buffer_load_b128(rx, (lane & 16) ? OOB : vo, 1024, XAUX);
}
__device__ __forceinline__ void ln_slice(const XRegs& xr, int lane, int i, unsigned short* dst) {
  float s = 0.f;
#pragma unroll
  for (int t = 0; t < 3; ++t)
#pragma unroll
    for (int k = 0; k < 4; ++k) s += bflo(xr.c[t][k]) + bfhi(xr.c[t][k]);
  s = x16_sum(row_sum(s));
  const float mean = s * (1.0f / D);
  float ss = 0.f, s2 = 0.f;
#pragma unroll
  for (int t = 0; t < 2; ++t)
#pragma unroll
    for (int k = 0; k < 4; ++k) { const float d0 = bflo(xr.c[t][k]) - mean, d1 = bfhi(xr.c[t][k]) - mean; ss = fmaf(d0, d0, ss); ss = fmaf(d1, d1, ss); }
#pragma unroll
  for (int k = 0; k < 4; ++k) { const float d0 = bflo(xr.c[2][k]) - mean, d1 = bfhi(xr.c[2][k]) - mean; s2 = fmaf(d0, d0, s2); s2 = fmaf(d1, d1, s2); }
  ss += (lane & 16) ? 0.f : s2;
  ss = x16_sum(row_sum(ss));
  const float mb = rbf(mean), den = rbf(sqrtf(rbf(rbf(ss * (1.0f / D)) + 1e-5f)));
  const int rel = ((lane & 31) - 10 * i) & 31;
  const int cidx = 10 * i + rel;
  const v4u m = cidx < 32 ? xr.c[0] : cidx < 64 ? xr.c[1] : xr.c[2];
  v4u o;
#pragma unroll
  for (int k = 0; k < 4; ++k) o[k] = cvtpk(rbf(bflo(m[k]) - mb) / den, rbf(bfhi(m[k]) - mb) / den);
  if (rel < 10) *reinterpret_cast<v4u*>(dst + (lane >> 5) * 80 + rel * 8) = o;
  asm volatile("s_waitcnt lgkmcnt(0)" ::: "memory");
}

__global__ void __launch_bounds__(512, 2)
fused_decode(const unsigned short* __restrict__ x, const unsigned short* __restrict__ kc, const unsigned short* __restrict__ vc,
             const unsigned short* __restrict__ wq, const unsigned short* __restrict__ wo, const unsigned short* __restrict__ w1,
             const unsigned short* __restrict__ w2, unsigned short* __restrict__ out, unsigned char* __restrict__ ws) {
  __shared__ Lds L;
  const int tid = threadIdx.x, lane = tid & 63, wave = __builtin_amdgcn_readfirstlane(tid >> 6);
  const int bid = blockIdx.x, xcd = bid & 7, sl = bid >> 3, kq = sl >> 3, h = xcd * 4 + kq, i = sl & 7, b = i >> 2, j = i & 3;
  const int gi = kq * 8 + xcd;
  const bool ffn = gi < NG;
  const int g = lane >> 4, n = lane & 15;
  const rsrc_t rws = mkrsrc(ws, WS_END);
  unsigned st_[16] = {0, 0, 0, 0, 0, 0, 0, 0, 0, 0, 0, 0, 0, 0, 0, 0};
  st_[15] = (unsigned)__builtin_amdgcn_s_memrealtime(); SB();
  asm volatile("" :: "s"(x), "s"(kc), "s"(vc), "s"(wq), "s"(wo), "s"(w1), "s"(w2), "s"(out), "s"(ws));
  STAMP(0);
  const size_t kvbase = ((size_t)(b * H + h) * SK + (SK - WIN) + j * 512) * DH;
  const rsrc_t rk = mkrsrc(kc + kvbase, 131072);
  const int lo = wave * 1024 + lane * 16;
  v4u K[32];
  const int a = wave - 4;
  const bool st1 = a == 0 || (a == 1 && ffn);
  unsigned ep = 0; XRegs xr; v4u W[20];
  if (st1) {
    __builtin_amdgcn_s_setprio(3);
    const rsrc_t rx = mkrsrc(x, 2 * D * 2);
    STAMP(11);
    x_issue(xr, rx, lane);
    ep = __builtin_amdgcn_raw_buffer_load_b32(rws, WS_EPOCH, 0, AUX_SC1);
    STAMP(12);
    if (a == 0) {
      const rsrc_t rw = mkrsrc(wq, D * H * DH * 2);
      const int vo = (80 * i + 20 * g) * 8192 + h * 256 + n * 16;
#pragma unroll
      for (int t = 0; t < 20; ++t) W[t] = ld_nt(rw, vo, t * 8192);
    } else {
      const rsrc_t rw = mkrsrc(w1, D * DFF * 2);
      const int vo = (80 * i + 20 * g + (n >> 3)) * (DFF * 2) + gi * 128 + (n & 7) * 16;
#pragma unroll
      for (int u = 0; u < 10; ++u) W[u] = ld_nt(rw, vo, u * (DFF * 4));
    }
    SB();
    STAMP(10);
  } else if (wave < 4) {
#pragma unroll
    for (int t = 0; t < KPRE; ++t) K[t] = ld_nt(rk, lo, t * 4096);
  }
  int wave2 = wave;
  asm volatile("" : "+s"(wave2));
  SB();
  if (__builtin_expect(wave2 < 4, 1)) {
    const rsrc_t rv = mkrsrc(vc + kvbase, 131072);
    v4u V[32];
    STAMP(10);
    __builtin_amdgcn_s_sleep(KDELAY);
    STAMP(11);
#pragma unroll
    for (int t = KPRE; t < 32; ++t) { if (t >= KWIN) VMCNT(KWIN - 1); if (t == KWIN) STAMP(12); if (t == 31) STAMP(13); K[t] = ld_nt(rk, lo, t * 4096); SB(); }
#pragma unroll
    for (int t = 0; t < VPRE; ++t) { VMCNT(KWIN - 1); V[t] = ld_nt(rv, lo, t * 4096); SB(); }
    unsigned sel[4];
#pragma unroll
    for (int jj = 0; jj < 4; ++jj) sel[jj] = psel(n & 7, n >> 3, jj, true);
    STAMP(1);
    wg_barrier();
    STAMP(2);
#pragma unroll
    for (int t = VPRE; t < VRING; ++t) { V[t] = ld_nt(rv, lo, t * 4096); SB(); }
    const v4u qv = *reinterpret_cast<const v4u*>(&L.qh[0][n * 8]);
    const unsigned tag = L.tag;
    float xs[32];
#pragma unroll
    for (int t = 0; t < 32; ++t) { float d = dot2(K[t].x, qv.x, 0.f); d = dot2(K[t].y, qv.y, d); d = dot2(K[t].z, qv.z, d); xs[t] = dot2(K[t].w, qv.w, d); }
#define TR_STEP(HALF, CTRL, BIT) _Pragma("unroll") for (int u = 0; u < (HALF); ++u) { \
      const float lo_ = xs[u], hi_ = xs[u + (HALF)]; const float t1_ = lo_ + dppf<CTRL>(lo_), t2_ = hi_ + dppf<CTRL>(hi_); xs[u] = (n & (BIT)) ? t2_ : t1_; }
    TR_STEP(16, ROR8, 8) TR_STEP(8, HMIR, 4) TR_STEP(4, XOR2, 2) TR_STEP(2, XOR1, 1)
#undef TR_STEP
    const float s0 = rbf(rbf(xs[0]) * 0.08838834764831845f), s1 = rbf(rbf(xs[1]) * 0.08838834764831845f);
    const float m = x32_max(x16_max(row_max(fmaxf(s0, s1))));
    const float p0 = __builtin_amdgcn_exp2f((s0 - m) * 1.4426950408889634f), p1 = __builtin_amdgcn_exp2f((s1 - m) * 1.4426950408889634f);
    const unsigned P0 = cvtpk(p0, p0 - rbf(p0)), P1 = cvtpk(p1, p1 - rbf(p1));
    const float lsum = x32_sum(x16_sum(row_sum(p0 + p1)));
    STAMP(3);
    f32x4 acc = {0.f, 0.f, 0.f, 0.f};
#define PV(t) { const unsigned bc = dppu<NEWB + ((t) >> 1)>(((t) & 1) ? P1 : P0); acc = mfma32(V[t], perm4(bc, sel), acc); \
      if ((t) + VRING < 32) V[((t) + VRING) & 31] = ld_nt(rv, lo, ((t) + VRING) * 4096); SB(); \
      if ((t) + VRING == 31) { STAMP(4); wg_barrier(); } }
    PV(0) PV(1) PV(2) PV(3) PV(4) PV(5) PV(6) PV(7) PV(8) PV(9) PV(10) PV(11) PV(12) PV(13) PV(14) PV(15)
    PV(16) PV(17) PV(18) PV(19) PV(20) PV(21) PV(22) PV(23) PV(24) PV(25) PV(26) PV(27) PV(28) PV(29) PV(30) PV(31)
#undef PV
#pragma unroll
    for (int k = 0; k < 4; ++k) acc[k] += dppf<ROR8>(acc[k]);
    if (n < 8) *reinterpret_cast<f32x4*>(&L.acc[wave][(g * 8 + n) * 4]) = acc;
    if (lane == 0) { L.ml[wave] = m; L.ml[4 + wave] = lsum; }
    STAMP(5);
    wg_barrier();
    STAMP(6);
    if (wave == 0) {
      const f32x4 mm = *reinterpret_cast<const f32x4*>(&L.ml[0]), ll = *reinterpret_cast<const f32x4*>(&L.ml[4]);
      f32x2 a[4];
#pragma unroll
      for (int w = 0; w < 4; ++w) a[w] = *reinterpret_cast<const f32x2*>(&L.acc[w][2 * lane]);
      const float M = fmaxf(fmaxf(mm[0], mm[1]), fmaxf(mm[2], mm[3]));
      float v0 = 0.f, v1 = 0.f, Ls = 0.f;
#pragma unroll
      for (int w = 0; w < 4; ++w) { const float f = __builtin_amdgcn_exp2f((mm[w] - M) * 1.4426950408889634f); v0 = fmaf(f, a[w][0], v0); v1 = fmaf(f, a[w][1], v1); Ls = fmaf(f, ll[w], Ls); }
      const int ap = WS_AP + (h * 8 + i) * 1280;
      st_unit(rws, ap + lane * 16, v0, v1, tag);
      if (lane == 0) st_unit(rws, ap + 1024, M, Ls, tag);
    }
    STAMP(7);
    if (wave == PROBE_W && lane == 0) L.probe = st_[PROBE_K] - st_[15];
    wg_barrier();
    STAMP(8);
    {
      const int kind = tid >> 7, col = tid & 127;
      if (col < 80 && (kind == 0 || ffn)) {
        const float* src = reinterpret_cast<const float*>(kind ? &L.df[0][0] : &L.ds[0][0]) + ((col >> 5) * 16 + (col & 7)) * 4 + ((col >> 3) & 3);
        float v0 = 0.f, v1 = 0.f;
#pragma unroll
        for (int a = 0; a < 4; ++a) { v0 += src[a * 256]; v1 += src[a * 256 + 32]; }
        st_unit(rws, (kind ? WS_FP + (i * 32 + gi) * 1280 : WS_SP + (i * 32 + h) * 1280) + col * 16, v0, v1, tag);
      }
    }
    STAMP(9);
    STAMP_FLUSH();
  } else {
    __builtin_amdgcn_s_setprio(3);
    unsigned selE[4], selO[4];
#pragma unroll
    for (int jj = 0; jj < 4; ++jj) { selE[jj] = psel(n & 7, 0, jj, true); selO[jj] = psel(n & 7, 1, jj, true); }
    if (st1) {
      const unsigned tag = ep + 1u;
      asm volatile("" :: "v"(xr.c[0]), "v"(xr.c[2])); STAMP(13); asm volatile("" :: "v"(ep)); STAMP(14);
      ln_slice(xr, lane, i, &L.xn[a][0][0]);
      v2u xw[5];
#pragma unroll
      for (int u = 0; u < 5; ++u) xw[u] = *reinterpret_cast<const v2u*>(&L.xn[a][n >> 3][20 * g + 4 * u]);
      STAMP(1);
      f32x4 dq = {0.f, 0.f, 0.f, 0.f};
      if (a == 0) {
#pragma unroll
        for (int t = 0; t < 20; ++t) { const unsigned src = (t & 2) ? xw[t >> 2].y : xw[t >> 2].x; dq = mfma32(W[t], perm4(src, (t & 1) ? selO : selE), dq); }
      } else {
        f32x4 d1 = {0.f, 0.f, 0.f, 0.f};
#pragma unroll
        for (int u = 0; u < 10; ++u) { const unsigned src = (u & 1) ? xw[u >> 1].y : xw[u >> 1].x; dq = mfma32(W[u], perm4(src, selE), dq); d1 = mfma32(W[u], perm4(src, selO), d1); }
#pragma unroll
        for (int k = 0; k < 4; ++k) dq[k] = x32_sum(g < 2 ? dq[k] : d1[k]);
      }
      const int po = a ? WS_HP + (gi * 8 + i) * 1024 + (n >> 3) * 512 : WS_QP + ((h * 8 + i) * 2 + (n >> 3)) * 1024;
      if (!a || g < 2) { const int qo = po + (g * 8 + (n & 7)) * 32; st_unit(rws, qo, dq[0], dq[1], tag); st_unit(rws, qo + 16, dq[2], dq[3], tag); }
      STAMP(2);
      __builtin_amdgcn_s_sleep(GATHER_SLEEP);
      const int go = (a ? WS_HP + gi * 8192 : WS_QP + (h * 16 + b) * 1024) + lane * 16, gs = a ? 1024 : 2048;
      v4u G[8];
      for (unsigned spin = 0;; ++spin) {
        MEMBAR();
#pragma unroll
        for (int p = 0; p < 8; ++p) G[p] = ld_sc1(rws, go, p * gs);
        bool ok = true;
#pragma unroll
        for (int p = 0; p < 8; ++p) ok &= unit_ok(G[p], tag);
        NPOLL(0);
        if (__all(ok) || spin > SPIN_CAP) break;
        __builtin_amdgcn_s_sleep(8);
      }
      float q0 = 0.f, q1 = 0.f;
#pragma unroll
      for (int p = 0; p < 8; ++p) { q0 += ux(G[p]); q1 += uz(G[p]); }
      if (a) { q0 = fmaxf(q0, 0.f); q1 = fmaxf(q1, 0.f); }
      const unsigned qq = cvtpk(q0, q1);
      const int d0 = 32 * g + 16 * (lane & 1) + ((lane >> 1) & 7);
      L.qh[a][d0] = (unsigned short)qq; L.qh[a][d0 + 8] = (unsigned short)(qq >> 16);
      if (tid == 256) L.tag = tag;
      STAMP(3);
    }
    wg_barrier();
    const unsigned tag = L.tag;
    const v2u hw = *reinterpret_cast<const v2u*>(&L.qh[1][(n >> 3) * 64 + 16 * a + 4 * g]);
    unsigned xres = 0;
    if (a == 0 && h < 20 && lane < 8) xres = x[(lane >> 2) * D + i * 80 + h * 4 + (lane & 3)];
    wg_barrier();
    STAMP(4);
    const rsrc_t r2 = mkrsrc(w2, DFF * D * 2), ro = mkrsrc(wo, H * DH * D * 2);
    v4u W2[4], WO[8];
    {
      const int v2o = (n < 10 && ffn) ? (64 * gi + 16 * a + 4 * g) * 1280 + i * 160 + n * 16 : OOB;
      const int voo = n < 10 ? (128 * h + 32 * a + 2 * g) * 1280 + i * 160 + n * 16 : OOB;
#pragma unroll
      for (int t = 0; t < 4; ++t) W2[t] = ld_nt(r2, v2o, t * 1280);
#pragma unroll
      for (int t = 0; t < 8; ++t) WO[t] = ld_nt(ro, voo, (8 * (t & 3) + (t >> 2)) * 1280);
      SB();
    }
    wg_barrier();
    f32x4 df = {0.f, 0.f, 0.f, 0.f};
    df = mfma32(W2[0], perm4(hw.x, selE), df); df = mfma32(W2[1], perm4(hw.x, selO), df);
    df = mfma32(W2[2], perm4(hw.y, selE), df); df = mfma32(W2[3], perm4(hw.y, selO), df);
    L.df[a][lane] = df;
    STAMP(5);
    v4u A0, A1, AM;
    asm volatile("" :: "v"(WO[7]));
    STAMP(12);
    {
      const int po = WS_AP + (h * 8 + (n >> 3) * 4 + (n & 3)) * 1280;
      const int uo = po + (16 * a + 4 * g + 2 * ((n >> 2) & 1)) * 16;
      for (unsigned spin = 0;; ++spin) {
        MEMBAR();
        A0 = ld_sc1(rws, uo, 0); A1 = ld_sc1(rws, uo, 16); AM = ld_sc1(rws, po, 1024);
        NPOLL(1);
        if (__all(unit_ok(A0, tag) & unit_ok(A1, tag) & unit_ok(AM, tag)) || spin > SPIN_CAP) break;
        __builtin_amdgcn_s_sleep(2);
      }
    }
    STAMP(6);
    {
      float M = ux(AM); M = fmaxf(M, dppf<XOR1>(M)); M = fmaxf(M, dppf<XOR2>(M));
      const float w = __builtin_amdgcn_exp2f((ux(AM) - M) * 1.4426950408889634f);
      float Ls = w * uz(AM); Ls += dppf<XOR1>(Ls); Ls += dppf<XOR2>(Ls);
      const float wn = w / Ls;
      float o0 = wn * ux(A0), o1 = wn * uz(A0), o2 = wn * ux(A1), o3 = wn * uz(A1);
      o0 += dppf<XOR1>(o0); o1 += dppf<XOR1>(o1); o2 += dppf<XOR1>(o2); o3 += dppf<XOR1>(o3);
      o0 += dppf<XOR2>(o0); o1 += dppf<XOR2>(o1); o2 += dppf<XOR2>(o2); o3 += dppf<XOR2>(o3);
      const unsigned m01 = cvtpk(o0, o1), m23 = cvtpk(o2, o3), t01 = dppu<HMIR>(m01), t23 = dppu<HMIR>(m23);
      const bool hf = (n & 4) != 0;
      const unsigned ow[4] = {hf ? t01 : m01, hf ? t23 : m23, hf ? m01 : t01, hf ? m23 : t23};
      f32x4 ds = {0.f, 0.f, 0.f, 0.f};
#pragma unroll
      for (int t = 0; t < 8; ++t) ds = mfma32(WO[t], perm4(ow[t >> 1], (t & 1) ? selO : selE), ds);
      L.ds[a][lane] = ds;
    }
    STAMP(7);
    wg_barrier();
    if (a == 0 && h < 20) {
      const int p = lane >> 2, co = (h * 4 + (lane & 3)) * 16;
      const int so = WS_SP + (i * 32 + p) * 1280 + co, fo = WS_FP + (i * 32 + p) * 1280 + co;
      const bool f2 = p + 16 < NG;
      v4u S0, S1, F0, F1;
      for (unsigned spin = 0;; ++spin) {
        MEMBAR();
        S0 = ld_sc1(rws, so, 0); S1 = ld_sc1(rws, so, 16 * 1280); F0 = ld_sc1(rws, fo, 0); F1 = ld_sc1(rws, f2 ? fo : OOB, 16 * 1280);
        NPOLL(2);
        if (__all(unit_ok(S0, tag) & unit_ok(S1, tag) & unit_ok(F0, tag) & (!f2 | unit_ok(F1, tag))) || spin > SPIN_CAP) break;
        __builtin_amdgcn_s_sleep(2);
      }
      STAMP(8);
      float r[4] = {ux(S0) + ux(S1), uz(S0) + uz(S1), ux(F0) + ux(F1), uz(F0) + uz(F1)};
#pragma unroll
      for (int k = 0; k < 4; ++k) { float t = r[k]; t += dppf<ROR8>(t); t += dppf<ROR4>(t); r[k] = x32_sum(x16_sum(t)); }
      const float sv = rbf((lane & 4) ? r[1] : r[0]), fv = rbf((lane & 4) ? r[3] : r[2]);
      const float gv = rbf(fv / (1.0f + __expf(-fv)));
      const float o = bflo(xres) + rbf(sv * gv);
      if (lane < 8) out[(lane >> 2) * D + i * 80 + h * 4 + (lane & 3)] = (unsigned short)cvtpk(o, o);
      if (bid == 0 && lane == 0) __builtin_amdgcn_raw_buffer_store_b32(tag, rws, WS_EPOCH, 0, AUX_SC1);
      STAMP(9);
      if (bid == PROBE_B) {
        const unsigned d = PROBE_W == 4 ? st_[PROBE_K] - st_[15] : L.probe;
        const unsigned until = st_[15] + 4000u + d;
        while ((int)((unsigned)__builtin_amdgcn_s_memrealtime() - until) < 0) __builtin_amdgcn_s_sleep(1);
      }
    }
    STAMP_FLUSH();
  }
}
}

extern "C" void kernel_launch(void* const* d_in, const int* in_sizes, int n_in, void* d_out, int out_size, void* d_ws, size_t ws_size, hipStream_t stream) {
  fused_decode<<<256, 512, 0, stream>>>((const unsigned short*)d_in[0], (const unsigned short*)d_in[1], (const unsigned short*)d_in[2],
                                        (const unsigned short*)d_in[3], (const unsigned short*)d_in[4], (const unsigned short*)d_in[6],
                                        (const unsigned short*)d_in[7], (unsigned short*)d_out, (unsigned char*)d_ws);
}
```

```cpp
#include <hip/hip_runtime.h>
#include <hip/hip_bf16.h>
#include <stdint.h>

namespace {
typedef unsigned v4u __attribute__((ext_vector_type(4)));
typedef unsigned v2u __attribute__((ext_vector_type(2)));
typedef float f32x4 __attribute__((ext_vector_type(4)));
typedef __bf16 bf16x2 __attribute__((ext_vector_type(2)));
typedef __bf16 bf16x8 __attribute__((ext_vector_type(8)));
typedef __amdgpu_buffer_rsrc_t rsrc_t;

constexpr int D = 640, H = 32, SK = 8192, DH = 128, WIN = 2048, DFF = 1728, NG = 27;
constexpr int AUX_NT = 2, AUX_SC1 = 16, RSRC3 = 0x00020000;
constexpr int OOB = 0x7ffffff0;
constexpr int WS_EPOCH = 0;
constexpr int WS_QP = 4096;
constexpr int WS_HP = WS_QP + 32 * 8 * 1024;
constexpr int WS_AP = WS_HP + 32 * 8 * 512;
constexpr int WS_SP = WS_AP + 32 * 8 * 640;
constexpr int WS_FP = WS_SP + 8 * 32 * 24 * 128;
constexpr int WS_END = WS_FP + 8 * 32 * 768;
constexpr unsigned SPIN_CAP = 1u << 18;
constexpr float LOG2E = 1.4426950408889634f;
#define GATHER_SLEEP 40
#define XAUX 0
#define KWIN 12
#define KPRE 2
#define KDELAY 10
#define CODE_LINES 96
#define VPRE 20
#define VRING 22
#define POLL1_AT -2
#define P2AUX 17
#define STAUX AUX_SC1
#define P2SLEEP 2
#define P2FIRST 20
#define P1SLEEP 2
#define EARLY1 -1
#define EARLY_SLEEP 0
#define PREW 1
#define P1FIRST 16
#define P1AUX 17
#define STAMP(k) do { } while (0)
#define STAMP_FLUSH() do { } while (0)
#define NPOLL(k) do { } while (0)

#define SB() __builtin_amdgcn_sched_barrier(0)
#define MEMBAR() asm volatile("" ::: "memory")
#define VMCNT(n) asm volatile("s_waitcnt vmcnt(%0)" ::"n"(n) : "memory")
__device__ __forceinline__ void wg_barrier() { SB(); asm volatile("s_waitcnt lgkmcnt(0)\n\ts_barrier" ::: "memory"); SB(); }
__device__ __forceinline__ float bflo(unsigned u) { return __uint_as_float(u << 16); }
__device__ __forceinline__ float bfhi(unsigned u) { return __uint_as_float(u & 0xffff0000u); }
__device__ __forceinline__ unsigned cvtpk(float lo, float hi) { bf16x2 v = {(__bf16)lo, (__bf16)hi}; return __builtin_bit_cast(unsigned, v); }
__device__ __forceinline__ float rbf(float f) { return __uint_as_float(cvtpk(f, f) & 0xffff0000u); }
template <int CTRL> __device__ __forceinline__ float dppf(float x) {
  return __builtin_bit_cast(float, __builtin_amdgcn_mov_dpp(__builtin_bit_cast(int, x), CTRL, 0xf, 0xf, true));
}
template <int CTRL> __device__ __forceinline__ unsigned dppu(unsigned x) { return (unsigned)__builtin_amdgcn_mov_dpp((int)x, CTRL, 0xf, 0xf, true); }
constexpr int XOR1 = 0xB1, XOR2 = 0x4E, HMIR = 0x141, ROR8 = 0x128, ROR4 = 0x124, NEWB = 0x150;
__device__ __forceinline__ float x16_sum(float x) { auto s = __builtin_amdgcn_permlane16_swap(__float_as_uint(x), __float_as_uint(x), false, false); return __uint_as_float(s[0]) + __uint_as_float(s[1]); }
__device__ __forceinline__ float x32_sum(float x) { auto s = __builtin_amdgcn_permlane32_swap(__float_as_uint(x), __float_as_uint(x), false, false); return __uint_as_float(s[0]) + __uint_as_float(s[1]); }
__device__ __forceinline__ float x16_max(float x) { auto s = __builtin_amdgcn_permlane16_swap(__float_as_uint(x), __float_as_uint(x), false, false); return fmaxf(__uint_as_float(s[0]), __uint_as_float(s[1])); }
__device__ __forceinline__ float x32_max(float x) { auto s = __builtin_amdgcn_permlane32_swap(__float_as_uint(x), __float_as_uint(x), false, false); return fmaxf(__uint_as_float(s[0]), __uint_as_float(s[1])); }
__device__ __forceinline__ float row_sum(float x) { x += dppf<XOR1>(x); x += dppf<XOR2>(x); x += dppf<HMIR>(x); x += dppf<ROR8>(x); return x; }
__device__ __forceinline__ float row_max(float x) { x = fmaxf(x, dppf<XOR1>(x)); x = fmaxf(x, dppf<XOR2>(x)); x = fmaxf(x, dppf<HMIR>(x)); x = fmaxf(x, dppf<ROR8>(x)); return x; }
__device__ __forceinline__ float dot2(unsigned k, unsigned q, float acc) {
  return __builtin_amdgcn_fdot2_f32_bf16(__builtin_bit_cast(bf16x2, k), __builtin_bit_cast(bf16x2, q), acc, false);
}
__device__ __forceinline__ f32x4 mfma32(v4u a, v4u b, f32x4 c) {
  return __builtin_amdgcn_mfma_f32_16x16x32_bf16(__builtin_bit_cast(bf16x8, a), __builtin_bit_cast(bf16x8, b), c, 0, 0, 0);
}
__device__ __forceinline__ rsrc_t mkrsrc(const void* p, unsigned bytes) { return __builtin_amdgcn_make_buffer_rsrc((void*)p, 0, bytes, RSRC3); }
__device__ __forceinline__ v4u ld_nt(rsrc_t r, int voff, int soff) { return __builtin_amdgcn_raw_buffer_load_b128(r, voff, soff, AUX_NT); }
__device__ __forceinline__ v4u ld_sc1(rsrc_t r, int voff, int soff) { return __builtin_amdgcn_raw_buffer_load_b128(r, voff, soff, AUX_SC1); }
__device__ __forceinline__ void st_unit(rsrc_t r, int voff, f32x4 v, unsigned tag) {
  v4u u;
#pragma unroll
  for (int k = 0; k < 4; ++k) u[k] = (__float_as_uint(v[k]) & 0xffffff00u) | tag;
  __builtin_amdgcn_raw_buffer_store_b128(u, r, voff, 0, STAUX);
}
__device__ __forceinline__ unsigned unit_bad(v4u g, unsigned tag) { return (g.x ^ tag) | (g.y ^ tag) | (g.z ^ tag) | (g.w ^ tag); }
__device__ __forceinline__ f32x4 unit_val(v4u g) {
  return f32x4{__uint_as_float(g.x & 0xffffff00u), __uint_as_float(g.y & 0xffffff00u), __uint_as_float(g.z & 0xffffff00u), __uint_as_float(g.w & 0xffffff00u)};
}
__device__ __forceinline__ unsigned psel(int e, int sh, int jj) {
  const unsigned pair = (unsigned)(2 * sh) | ((unsigned)(2 * sh + 1) << 8);
  const unsigned s = (e & 1) ? ((pair << 16) | 0x0c0cu) : (0x0c0c0000u | pair);
  return (e >> 1) == jj ? s : 0x0c0c0c0cu;
}
__device__ __forceinline__ unsigned perm(unsigned src, unsigned sel) { return __builtin_amdgcn_perm(src, src, sel); }
__device__ __forceinline__ v4u perm4(unsigned src, const unsigned (&s)[4]) { return v4u{perm(src, s[0]), perm(src, s[1]), perm(src, s[2]), perm(src, s[3])}; }

struct __attribute__((aligned(16))) Lds {
  f32x4 df[4][64];
  f32x4 acc[4][32];
  float ml[8];
  unsigned short xn[2][2][80];
  unsigned short qh[2][128];
  unsigned tag; unsigned pad[3];
};

struct XRegs { v4u c[3]; };
__device__ __forceinline__ void x_issue(XRegs& xr, rsrc_t rx, int lane) {
  const int vo = (lane >> 5) * 1280 + (lane & 31) * 16;
  xr.c[0] = __builtin_amdgcn_raw_buffer_load_b128(rx, vo, 0, XAUX);
  xr.c[1] = __builtin_amdgcn_raw_buffer_load_b128(rx, vo, 512, XAUX);
  xr.c[2] = __builtin_amdgcn_raw_buffer_load_b128(rx, (lane & 16) ? OOB : vo, 1024, XAUX);
}
__device__ __forceinline__ void ln_slice(const XRegs& xr, int lane, int i, unsigned short* dst) {
  float sa = 0.f, sb = 0.f, sc = 0.f;
#pragma unroll
  for (int k = 0; k < 4; ++k) { sa = dot2(xr.c[0][k], 0x3f803f80u, sa); sb = dot2(xr.c[1][k], 0x3f803f80u, sb); sc = dot2(xr.c[2][k], 0x3f803f80u, sc); }
  const float mean = x16_sum(row_sum(sa + sb + sc)) * (1.0f / D);
  float q0 = 0.f, q1 = 0.f, q2 = 0.f, q3 = 0.f, q4 = 0.f, q5 = 0.f;
#pragma unroll
  for (int k = 0; k < 4; ++k) {
    const float a0 = bflo(xr.c[0][k]) - mean, a1 = bfhi(xr.c[0][k]) - mean, b0 = bflo(xr.c[1][k]) - mean, b1 = bfhi(xr.c[1][k]) - mean;
    const float c0 = bflo(xr.c[2][k]) - mean, c1 = bfhi(xr.c[2][k]) - mean;
    q0 = fmaf(a0, a0, q0); q1 = fmaf(a1, a1, q1); q2 = fmaf(b0, b0, q2); q3 = fmaf(b1, b1, q3); q4 = fmaf(c0, c0, q4); q5 = fmaf(c1, c1, q5);
  }
  float ss = (q0 + q1) + (q2 + q3) + ((lane & 16) ? 0.f : q4 + q5);
  ss = x16_sum(row_sum(ss));
  const float mb = rbf(mean), den = rbf(__builtin_amdgcn_sqrtf(rbf(rbf(ss * (1.0f / D)) + 1e-5f)));
  const float rden = __builtin_amdgcn_rcpf(den);
  const int rel = ((lane & 31) - 10 * i) & 31;
  const int cidx = 10 * i + rel;
  const v4u m = cidx < 32 ? xr.c[0] : cidx < 64 ? xr.c[1] : xr.c[2];
  v4u o;
#pragma unroll
  for (int k = 0; k < 4; ++k) { const unsigned d = cvtpk(bflo(m[k]) - mb, bfhi(m[k]) - mb); o[k] = cvtpk(bflo(d) * rden, bfhi(d) * rden); }
  if (rel < 10) *reinterpret_cast<v4u*>(dst + (lane >> 5) * 80 + rel * 8) = o;
  asm volatile("s_waitcnt lgkmcnt(0)" ::: "memory");
}

__global__ void __launch_bounds__(512, 2) __attribute__((aligned(4096)))
fused_decode(const unsigned short* __restrict__ x, const unsigned short* __restrict__ kc, const unsigned short* __restrict__ vc,
             const unsigned short* __restrict__ wq, const unsigned short* __restrict__ wo, const unsigned short* __restrict__ w1,
             const unsigned short* __restrict__ w2, unsigned short* __restrict__ out, unsigned char* __restrict__ ws) {
  __shared__ Lds L;
  const int tid = threadIdx.x, lane = tid & 63, wave = __builtin_amdgcn_readfirstlane(tid >> 6);
  const int bid = blockIdx.x, xcd = bid & 7, sl = bid >> 3, kq = sl >> 3, h = xcd * 4 + kq, i = sl & 7, b = i >> 2, j = i & 3;
  const int gi = kq * 8 + xcd;
  const bool ffn = gi < NG;
  const int g = lane >> 4, n = lane & 15;
  const rsrc_t rws = mkrsrc(ws, WS_END);
  asm volatile("" :: "s"(x), "s"(kc), "s"(vc), "s"(wq), "s"(wo), "s"(w1), "s"(w2), "s"(out), "s"(ws));
  STAMP(0);
  const size_t kvbase = ((size_t)(b * H + h) * SK + (SK - WIN) + j * 512) * DH;
  const rsrc_t rk = mkrsrc(kc + kvbase, 131072);
  const int lo = wave * 1024 + lane * 16;
  v4u K[32];
  const int a = wave - 4;
  const bool st1 = a == 0 || (a == 1 && ffn);
  unsigned ep = 0; XRegs xr; v4u W[20];
  if (st1) {
    __builtin_amdgcn_s_setprio(3);
    const rsrc_t rx = mkrsrc(x, 2 * D * 2);
    x_issue(xr, rx, lane);
    ep = __builtin_amdgcn_raw_buffer_load_b32(rws, WS_EPOCH, 0, AUX_SC1);
    if (a == 0) {
      const rsrc_t rw = mkrsrc(wq, D * H * DH * 2);
      const int vo = (80 * i + 20 * g) * 8192 + h * 256 + n * 16;
#pragma unroll
      for (int t = 0; t < 20; ++t) W[t] = ld_nt(rw, vo, t * 8192);
    } else {
      const rsrc_t rw = mkrsrc(w1, D * DFF * 2);
      const int vo = (80 * i + 20 * g + (n >> 3)) * (DFF * 2) + gi * 128 + (n & 7) * 16;
#pragma unroll
      for (int u = 0; u < 10; ++u) W[u] = ld_nt(rw, vo, u * (DFF * 4));
    }
    SB();
    STAMP(10);
  } else if (wave < 4) {
#pragma unroll
    for (int t = 0; t < KPRE; ++t) K[t] = ld_nt(rk, lo, t * 4096);
  }
  else if (a >= 2) {
    unsigned long long pc;
    asm volatile("s_getpc_b64 %0" : "=s"(pc));
    pc &= ~4095ull;
    const int l0 = sl * 2 + (a - 2), l1 = l0 + 64 < CODE_LINES ? l0 + 64 : l0;
    const unsigned long long p0 = pc + (unsigned)(CODE_LINES - 1 - l0) * 128u, p1 = pc + (unsigned)(CODE_LINES - 1 - l1) * 128u;
    unsigned d0, d1;
    asm volatile("s_load_dword %0, %2, 0x0\n\ts_load_dword %1, %3, 0x0\n\ts_waitcnt lgkmcnt(0)" : "=&s"(d0), "=&s"(d1) : "s"(p0), "s"(p1));
  }
  int wave2 = wave;
  asm volatile("" : "+s"(wave2));
  SB();
  if (__builtin_expect(wave2 < 4, 1)) {
    const rsrc_t rv = mkrsrc(vc + kvbase, 131072);
    v4u V[32];
    STAMP(10);
    __builtin_amdgcn_s_sleep(KDELAY);
#pragma unroll
    for (int t = KPRE; t < 32; ++t) { if (t >= KWIN) VMCNT(KWIN - 1); if (t == KWIN) STAMP(12); if (t == 31) STAMP(13); K[t] = ld_nt(rk, lo, t * 4096); SB(); }
#pragma unroll
    for (int t = 0; t < VPRE; ++t) { VMCNT(KWIN - 1); V[t] = ld_nt(rv, lo, t * 4096); SB(); }
    unsigned sel[4];
#pragma unroll
    for (int jj = 0; jj < 4; ++jj) sel[jj] = psel(n & 7, n >> 3, jj);
    STAMP(1);
    wg_barrier();
    STAMP(2);
#pragma unroll
    for (int t = VPRE; t < VRING; ++t) { V[t] = ld_nt(rv, lo, t * 4096); SB(); }
    const v4u qv = *reinterpret_cast<const v4u*>(&L.qh[0][n * 8]);
    const unsigned tag = L.tag & 0xffu;
    float xs[32];
#pragma unroll
    for (int t = 0; t < 32; ++t) { float d = dot2(K[t].x, qv.x, 0.f); d = dot2(K[t].y, qv.y, d); d = dot2(K[t].z, qv.z, d); xs[t] = dot2(K[t].w, qv.w, d); }
#define TR_STEP(HALF, CTRL, BIT) _Pragma("unroll") for (int u = 0; u < (HALF); ++u) { \
      const float lo_ = xs[u], hi_ = xs[u + (HALF)]; const float t1_ = lo_ + dppf<CTRL>(lo_), t2_ = hi_ + dppf<CTRL>(hi_); xs[u] = (n & (BIT)) ? t2_ : t1_; }
    TR_STEP(16, ROR8, 8) TR_STEP(8, HMIR, 4) TR_STEP(4, XOR2, 2) TR_STEP(2, XOR1, 1)
#undef TR_STEP
    const float s0 = rbf(rbf(xs[0]) * 0.08838834764831845f), s1 = rbf(rbf(xs[1]) * 0.08838834764831845f);
    const float m = x32_max(x16_max(row_max(fmaxf(s0, s1))));
    const float p0 = __builtin_amdgcn_exp2f((s0 - m) * LOG2E), p1 = __builtin_amdgcn_exp2f((s1 - m) * LOG2E);
    const unsigned P0 = cvtpk(p0, p0 - rbf(p0)), P1 = cvtpk(p1, p1 - rbf(p1));
    const float lsum = x32_sum(x16_sum(row_sum(p0 + p1)));
    if (lane == 0) { L.ml[wave] = m; L.ml[4 + wave] = lsum; }
    STAMP(3);
    f32x4 acc = {0.f, 0.f, 0.f, 0.f};
#define PV(t) { const unsigned bc = dppu<NEWB + ((t) >> 1)>(((t) & 1) ? P1 : P0); acc = mfma32(V[t], perm4(bc, sel), acc); \
      if ((t) + VRING < 32) V[((t) + VRING) & 31] = ld_nt(rv, lo, ((t) + VRING) * 4096); SB(); \
      if ((t) + VRING == 31) { STAMP(4); if (EARLY1 < 0) wg_barrier(); } }
    PV(0) PV(1) PV(2) PV(3) PV(4) PV(5) PV(6) PV(7) PV(8) PV(9) PV(10) PV(11) PV(12) PV(13) PV(14) PV(15)
    PV(16) PV(17) PV(18) PV(19) PV(20) PV(21) PV(22) PV(23) PV(24) PV(25) PV(26) PV(27) PV(28) PV(29) PV(30) PV(31)
#undef PV
#pragma unroll
    for (int k = 0; k < 4; ++k) acc[k] += dppf<ROR8>(acc[k]);
    if (n < 8) L.acc[wave][g * 8 + n] = acc;
    STAMP(5);
    wg_barrier();
    STAMP(6);
    if (wave == 0) {
      const f32x4 mm = *reinterpret_cast<const f32x4*>(&L.ml[0]), ll = *reinterpret_cast<const f32x4*>(&L.ml[4]);
      f32x4 av[4];
#pragma unroll
      for (int w = 0; w < 4; ++w) av[w] = L.acc[w][lane & 31];
      const float M = fmaxf(fmaxf(mm[0], mm[1]), fmaxf(mm[2], mm[3]));
      f32x4 v = {0.f, 0.f, 0.f, 0.f}; float Ls = 0.f;
#pragma unroll
      for (int w = 0; w < 4; ++w) { const float f = __builtin_amdgcn_exp2f((mm[w] - M) * LOG2E); v += f * av[w]; Ls = fmaf(f, ll[w], Ls); }
      if (lane == 32) v = f32x4{M, Ls, M, Ls};
      if (lane <= 32) st_unit(rws, WS_AP + (h * 8 + i) * 640 + lane * 16, v, tag);
    }
    STAMP(7);
    wg_barrier();
    STAMP(8);
    if (wave == 1 && lane < 48 && ffn) {
      const f32x4* src = &L.df[0][lane];
      st_unit(rws, WS_FP + (i * 32 + gi) * 768 + lane * 16, (src[0] + src[64]) + (src[128] + src[192]), tag);
    }
    STAMP(9);
    STAMP_FLUSH();
  } else {
    __builtin_amdgcn_s_setprio(3);
    unsigned selE[4], selO[4];
#pragma unroll
    for (int jj = 0; jj < 4; ++jj) { selE[jj] = psel(n & 7, 0, jj); selO[jj] = psel(n & 7, 1, jj); }
    if (st1) {
      const unsigned tagw = ep + 1u, tag = tagw & 0xffu;
      ln_slice(xr, lane, i, &L.xn[a][0][0]);
      v2u xw[5];
#pragma unroll
      for (int u = 0; u < 5; ++u) xw[u] = *reinterpret_cast<const v2u*>(&L.xn[a][n >> 3][20 * g + 4 * u]);
      STAMP(1);
      f32x4 d0 = {0.f, 0.f, 0.f, 0.f}, d1 = {0.f, 0.f, 0.f, 0.f};
      if (a == 0) {
#pragma unroll
        for (int u = 0; u < 10; ++u) { const unsigned src = (u & 1) ? xw[u >> 1].y : xw[u >> 1].x; d0 = mfma32(W[2 * u], perm4(src, selE), d0); d1 = mfma32(W[2 * u + 1], perm4(src, selO), d1); }
        d0 += d1;
      } else {
#pragma unroll
        for (int u = 0; u < 10; ++u) { const unsigned src = (u & 1) ? xw[u >> 1].y : xw[u >> 1].x; d0 = mfma32(W[u], perm4(src, selE), d0); d1 = mfma32(W[u], perm4(src, selO), d1); }
#pragma unroll
        for (int k = 0; k < 4; ++k) d0[k] = x32_sum(g < 2 ? d0[k] : d1[k]);
      }
      const int po = a ? WS_HP + (gi * 8 + i) * 512 + (n >> 3) * 256 : WS_QP + (h * 8 + i) * 1024 + (n >> 3) * 512;
      if (!a || g < 2) st_unit(rws, po + (g * 8 + (n & 7)) * 16, d0, tag);
      STAMP(2);
      __builtin_amdgcn_s_sleep(GATHER_SLEEP);
      const int gs = a ? 512 : 1024;
      const int go = (a ? WS_HP + gi * 4096 : WS_QP + h * 8192 + b * 512) + (lane >> 5) * gs + (lane & 31) * 16;
      v4u G[4];
      for (unsigned spin = 0;; ++spin) {
        MEMBAR();
#pragma unroll
        for (int p = 0; p < 4; ++p) G[p] = ld_sc1(rws, go, p * 2 * gs);
        const unsigned bad = unit_bad(G[0], tag) | unit_bad(G[1], tag) | unit_bad(G[2], tag) | unit_bad(G[3], tag);
        NPOLL(0);
        if (__all((bad & 0xffu) == 0u) || spin > SPIN_CAP) break;
        __builtin_amdgcn_s_sleep(8);
      }
      f32x4 qv = (unit_val(G[0]) + unit_val(G[1])) + (unit_val(G[2]) + unit_val(G[3]));
#pragma unroll
      for (int k = 0; k < 4; ++k) { qv[k] = x32_sum(qv[k]); if (a) qv[k] = fmaxf(qv[k], 0.f); }
      const unsigned q01 = cvtpk(qv[0], qv[1]), q23 = cvtpk(qv[2], qv[3]);
      if (lane < 32) {
        unsigned short* dst = &L.qh[a][(lane >> 3) * 32 + (lane & 7)];
        dst[0] = (unsigned short)q01; dst[8] = (unsigned short)(q01 >> 16); dst[16] = (unsigned short)q23; dst[24] = (unsigned short)(q23 >> 16);
      }
      if (tid == 256) L.tag = tagw;
      STAMP(3);
    }
    wg_barrier();
    const unsigned tagw = L.tag, tag = tagw & 0xffu;
    const v2u hw = *reinterpret_cast<const v2u*>(&L.qh[1][(n >> 3) * 64 + 16 * a + 4 * g]);
    const int ocol = 8 * (4 * (gi >> 3) + ((lane >> 1) & 3)) + 2 * (gi & 3) + (lane & 1);
    const int oidx = ((gi >> 2) & 1) * D + i * 80 + ocol;
    const bool ow_ = a == 0 && gi < 24 && lane < 8 && ocol < 80;
    unsigned xres = 0;
    if (ow_) xres = x[oidx];
    if (a == 2 && lane < 40) st_unit(rws, WS_AP + (h * 8 + i) * 640 + lane * 16, f32x4{0.f, 0.f, 0.f, 0.f}, tag ^ 0x80u);
    const rsrc_t r2 = mkrsrc(w2, DFF * D * 2), ro = mkrsrc(wo, H * DH * D * 2);
    v4u W2[4], WO[8];
    const int v2o = (n < 10 && ffn) ? (64 * gi + 16 * a + 4 * g) * 1280 + i * 160 + n * 16 : OOB;
    const int voo = n < 10 ? (128 * h + 32 * a + 2 * g) * 1280 + i * 160 + n * 16 : OOB;
    VMCNT(0);
    wg_barrier();
#pragma unroll
    for (int t = 0; t < 4; ++t) W2[t] = ld_nt(r2, v2o, t * 1280);
#pragma unroll
    for (int t = 0; t < 8; ++t) WO[t] = ld_nt(ro, voo, (8 * (t & 3) + (t >> 2)) * 1280);
    SB();
    STAMP(4);
    wg_barrier();
    f32x4 df = {0.f, 0.f, 0.f, 0.f};
    df = mfma32(W2[0], perm4(hw.x, selE), df); df = mfma32(W2[1], perm4(hw.x, selO), df);
    df = mfma32(W2[2], perm4(hw.y, selE), df); df = mfma32(W2[3], perm4(hw.y, selO), df);
    L.df[a][lane] = df;
    STAMP(5);
    wg_barrier();
    const int apo = WS_AP + (h * 8 + (n >> 3) * 4 + (n & 3)) * 640;
    const int auo = apo + (8 * a + 2 * g + ((n >> 2) & 1)) * 16;
    v4u AU = {0, 0, 0, 0}, AM = {0, 0, 0, 0};
    if (P1FIRST) __builtin_amdgcn_s_sleep(P1FIRST);
    AU = __builtin_amdgcn_raw_buffer_load_b128(rws, auo, 0, P1AUX); AM = __builtin_amdgcn_raw_buffer_load_b128(rws, apo, 512, P1AUX);
    SB();
    STAMP(10);
    {
      bool nu = ((unit_bad(AU, tag) | unit_bad(AM, tag)) & 0xffu) != 0u;
      for (unsigned spin = 0; __any(nu) && spin <= SPIN_CAP; ++spin) {
        if (spin || POLL1_AT == -2) __builtin_amdgcn_s_sleep(P1SLEEP);
        MEMBAR();
        const v4u u_ = __builtin_amdgcn_raw_buffer_load_b128(rws, nu ? auo : OOB, 0, P1AUX), m_ = __builtin_amdgcn_raw_buffer_load_b128(rws, nu ? apo : OOB, 512, P1AUX);
        if (nu) { AU = u_; AM = m_; }
        nu = nu && ((unit_bad(u_, tag) | unit_bad(m_, tag)) & 0xffu) != 0u;
        NPOLL(1);
      }
    }
    STAMP(6);
    {
      const f32x4 am = unit_val(AM);
      float M = am[0]; M = fmaxf(M, dppf<XOR1>(M)); M = fmaxf(M, dppf<XOR2>(M));
      const float w = __builtin_amdgcn_exp2f((am[0] - M) * LOG2E);
      float Ls = w * am[1]; Ls += dppf<XOR1>(Ls); Ls += dppf<XOR2>(Ls);
      f32x4 o = unit_val(AU) * (w * __builtin_amdgcn_rcpf(Ls));
#pragma unroll
      for (int k = 0; k < 4; ++k) o[k] += dppf<XOR1>(o[k]);
#pragma unroll
      for (int k = 0; k < 4; ++k) o[k] += dppf<XOR2>(o[k]);
      const unsigned m01 = cvtpk(o[0], o[1]), m23 = cvtpk(o[2], o[3]), t01 = dppu<HMIR>(m01), t23 = dppu<HMIR>(m23);
      const bool hf = (n & 4) != 0;
      const unsigned ow[4] = {hf ? t01 : m01, hf ? t23 : m23, hf ? m01 : t01, hf ? m23 : t23};
      f32x4 ds = {0.f, 0.f, 0.f, 0.f}, dt = {0.f, 0.f, 0.f, 0.f};
#pragma unroll
      for (int t = 0; t < 4; ++t) { ds = mfma32(WO[2 * t], perm4(ow[t], selE), ds); dt = mfma32(WO[2 * t + 1], perm4(ow[t], selO), dt); }
      if (lane < 48) st_unit(rws, WS_SP + ((i * 32 + h) * 24 + (lane >> 1)) * 128 + a * 32 + (lane & 1) * 16, ds + dt, tag);
    }
    STAMP(7);
    if (a == 0 && gi < 24) {
      const int p = lane >> 1;
      const int so = WS_SP + ((i * 32 + (lane >> 3)) * 24 + gi) * 128 + (lane & 7) * 16;
      const int fo = WS_FP + (i * 32 + p) * 768 + gi * 32 + (lane & 1) * 16;
      v4u S[4], F0 = {0, 0, 0, 0};
      if (P2FIRST) __builtin_amdgcn_s_sleep(P2FIRST);
      bool nf = p < NG;
      for (unsigned spin = 0;; ++spin) {
        MEMBAR();
#pragma unroll
        for (int q = 0; q < 4; ++q) S[q] = __builtin_amdgcn_raw_buffer_load_b128(rws, so, q * (8 * 24 * 128), P2AUX);
        const v4u f_ = __builtin_amdgcn_raw_buffer_load_b128(rws, nf ? fo : OOB, 0, P2AUX);
        if (nf) F0 = f_;
        nf = nf && (unit_bad(f_, tag) & 0xffu) != 0u;
        const bool ns = ((unit_bad(S[0], tag) | unit_bad(S[1], tag) | unit_bad(S[2], tag) | unit_bad(S[3], tag)) & 0xffu) != 0u;
        NPOLL(2);
        if (!__any(ns | nf) || spin > SPIN_CAP) break;
        __builtin_amdgcn_s_sleep(P2SLEEP);
      }
      STAMP(8);
      const f32x4 rs = (unit_val(S[0]) + unit_val(S[1])) + (unit_val(S[2]) + unit_val(S[3])), rf = unit_val(F0);
      float s2[2], f2[2];
#pragma unroll
      for (int k = 0; k < 2; ++k) {
        const float a0 = rs[k] + dppf<ROR4>(rs[k]), a1 = rs[k + 2] + dppf<ROR4>(rs[k + 2]), b0 = rf[k] + dppf<ROR4>(rf[k]), b1 = rf[k + 2] + dppf<ROR4>(rf[k + 2]);
        const float a0r = a0 + dppf<ROR8>(a0), a1r = a1 + dppf<ROR8>(a1), b0r = b0 + dppf<ROR8>(b0), b1r = b1 + dppf<ROR8>(b1);
        s2[k] = (lane & 4) ? a1r : a0r; f2[k] = (lane & 4) ? b1r : b0r;
      }
      const float sa = s2[0] + dppf<XOR2>(s2[0]), sb = s2[1] + dppf<XOR2>(s2[1]), fa = f2[0] + dppf<XOR2>(f2[0]), fb = f2[1] + dppf<XOR2>(f2[1]);
      const float sv = rbf(x32_sum(x16_sum((lane & 2) ? sb : sa))), fv = rbf(x32_sum(x16_sum((lane & 2) ? fb : fa)));
      const float gv = rbf(fv * __builtin_amdgcn_rcpf(1.0f + __builtin_amdgcn_exp2f(-fv * LOG2E)));
      const float o = bflo(xres) + rbf(sv * gv);
      if (ow_) out[oidx] = (unsigned short)cvtpk(o, o);
      if (bid == 0 && lane == 0) __builtin_amdgcn_raw_buffer_store_b32(tagw, rws, WS_EPOCH, 0, AUX_SC1);
      STAMP(9);
    }
    STAMP_FLUSH();
  }
}
}

extern "C" void kernel_launch(void* const* d_in, const int* in_sizes, int n_in, void* d_out, int out_size, void* d_ws, size_t ws_size, hipStream_t stream) {
  fused_decode<<<256, 512, 0, stream>>>((const unsigned short*)d_in[0], (const unsigned short*)d_in[1], (const unsigned short*)d_in[2],
                                        (const unsigned short*)d_in[3], (const unsigned short*)d_in[4], (const unsigned short*)d_in[6],
                                        (const unsigned short*)d_in[7], (unsigned short*)d_out, (unsigned char*)d_ws);
}
```

```cpp
#include <hip/hip_runtime.h>
#include <hip/hip_bf16.h>
#include <stdint.h>

namespace {
typedef unsigned v4u __attribute__((ext_vector_type(4)));
typedef unsigned v2u __attribute__((ext_vector_type(2)));
typedef float f32x4 __attribute__((ext_vector_type(4)));
typedef __bf16 bf16x2 __attribute__((ext_vector_type(2)));
typedef __bf16 bf16x8 __attribute__((ext_vector_type(8)));
typedef __amdgpu_buffer_rsrc_t rsrc_t;

constexpr int D = 640, H = 32, SK = 8192, DH = 128, WIN = 2048, DFF = 1728, NG = 27;
constexpr int AUX_NT = 2, AUX_SC1 = 16, RSRC3 = 0x00020000;
constexpr int OOB = 0x7ffffff0;
constexpr int WS_EPOCH = 0;
constexpr int WS_QP = 4096;
constexpr int WS_HP = WS_QP + 32 * 8 * 1024;
constexpr int WS_AP = WS_HP + 32 * 8 * 512;
constexpr int WS_SP = WS_AP + 32 * 8 * 640;
constexpr int WS_FP = WS_SP + 8 * 32 * 24 * 128;
constexpr int WS_END = WS_FP + 8 * 32 * 768;
constexpr unsigned SPIN_CAP = 1u << 18;
constexpr float LOG2E = 1.4426950408889634f;
constexpr int KWIN = 12, KPRE = 2, KDELAY = 10, VPRE = 20, VRING = 22;
constexpr int GATHER_SLEEP = 40, P1FIRST = 16, P1SLEEP = 2, P2FIRST = 20, P2SLEEP = 2;
constexpr int AUX_SYS = 17;

#define SB() __builtin_amdgcn_sched_barrier(0)
#define MEMBAR() asm volatile("" ::: "memory")
#define VMCNT(n) asm volatile("s_waitcnt vmcnt(%0)" ::"n"(n) : "memory")
__device__ __forceinline__ void wg_barrier() { SB(); asm volatile("s_waitcnt lgkmcnt(0)\n\ts_barrier" ::: "memory"); SB(); }
__device__ __forceinline__ float bflo(unsigned u) { return __uint_as_float(u << 16); }
__device__ __forceinline__ float bfhi(unsigned u) { return __uint_as_float(u & 0xffff0000u); }
__device__ __forceinline__ unsigned cvtpk(float lo, float hi) { bf16x2 v = {(__bf16)lo, (__bf16)hi}; return __builtin_bit_cast(unsigned, v); }
__device__ __forceinline__ float rbf(float f) { return __uint_as_float(cvtpk(f, f) & 0xffff0000u); }
template <int CTRL> __device__ __forceinline__ float dppf(float x) {
  return __builtin_bit_cast(float, __builtin_amdgcn_mov_dpp(__builtin_bit_cast(int, x), CTRL, 0xf, 0xf, true));
}
template <int CTRL> __device__ __forceinline__ unsigned dppu(unsigned x) { return (unsigned)__builtin_amdgcn_mov_dpp((int)x, CTRL, 0xf, 0xf, true); }
constexpr int XOR1 = 0xB1, XOR2 = 0x4E, HMIR = 0x141, ROR8 = 0x128, ROR4 = 0x124, NEWB = 0x150;
__device__ __forceinline__ float x16_sum(float x) { auto s = __builtin_amdgcn_permlane16_swap(__float_as_uint(x), __float_as_uint(x), false, false); return __uint_as_float(s[0]) + __uint_as_float(s[1]); }
__device__ __forceinline__ float x32_sum(float x) { auto s = __builtin_amdgcn_permlane32_swap(__float_as_uint(x), __float_as_uint(x), false, false); return __uint_as_float(s[0]) + __uint_as_float(s[1]); }
__device__ __forceinline__ float x16_max(float x) { auto s = __builtin_amdgcn_permlane16_swap(__float_as_uint(x), __float_as_uint(x), false, false); return fmaxf(__uint_as_float(s[0]), __uint_as_float(s[1])); }
__device__ __forceinline__ float x32_max(float x) { auto s = __builtin_amdgcn_permlane32_swap(__float_as_uint(x), __float_as_uint(x), false, false); return fmaxf(__uint_as_float(s[0]), __uint_as_float(s[1])); }
__device__ __forceinline__ float row_sum(float x) { x += dppf<XOR1>(x); x += dppf<XOR2>(x); x += dppf<HMIR>(x); x += dppf<ROR8>(x); return x; }
__device__ __forceinline__ float row_max(float x) { x = fmaxf(x, dppf<XOR1>(x)); x = fmaxf(x, dppf<XOR2>(x)); x = fmaxf(x, dppf<HMIR>(x)); x = fmaxf(x, dppf<ROR8>(x)); return x; }
__device__ __forceinline__ float dot2(unsigned k, unsigned q, float acc) {
  return __builtin_amdgcn_fdot2_f32_bf16(__builtin_bit_cast(bf16x2, k), __builtin_bit_cast(bf16x2, q), acc, false);
}
__device__ __forceinline__ f32x4 mfma32(v4u a, v4u b, f32x4 c) {
  return __builtin_amdgcn_mfma_f32_16x16x32_bf16(__builtin_bit_cast(bf16x8, a), __builtin_bit_cast(bf16x8, b), c, 0, 0, 0);
}
__device__ __forceinline__ rsrc_t mkrsrc(const void* p, unsigned bytes) { return __builtin_amdgcn_make_buffer_rsrc((void*)p, 0, bytes, RSRC3); }
__device__ __forceinline__ v4u ld_nt(rsrc_t r, int voff, int soff) { return __builtin_amdgcn_raw_buffer_load_b128(r, voff, soff, AUX_NT); }
__device__ __forceinline__ v4u ld_sc1(rsrc_t r, int voff, int soff) { return __builtin_amdgcn_raw_buffer_load_b128(r, voff, soff, AUX_SC1); }
__device__ __forceinline__ void st_unit(rsrc_t r, int voff, f32x4 v, unsigned tag) {
  v4u u;
#pragma unroll
  for (int k = 0; k < 4; ++k) u[k] = (__float_as_uint(v[k]) & 0xffffff00u) | tag;
  __builtin_amdgcn_raw_buffer_store_b128(u, r, voff, 0, AUX_SC1);
}
__device__ __forceinline__ unsigned unit_bad(v4u g, unsigned tag) { return (g.x ^ tag) | (g.y ^ tag) | (g.z ^ tag) | (g.w ^ tag); }
__device__ __forceinline__ f32x4 unit_val(v4u g) {
  return f32x4{__uint_as_float(g.x & 0xffffff00u), __uint_as_float(g.y & 0xffffff00u), __uint_as_float(g.z & 0xffffff00u), __uint_as_float(g.w & 0xffffff00u)};
}
__device__ __forceinline__ unsigned psel(int e, int sh, int jj) {
  const unsigned pair = (unsigned)(2 * sh) | ((unsigned)(2 * sh + 1) << 8);
  const unsigned s = (e & 1) ? ((pair << 16) | 0x0c0cu) : (0x0c0c0000u | pair);
  return (e >> 1) == jj ? s : 0x0c0c0c0cu;
}
__device__ __forceinline__ unsigned perm(unsigned src, unsigned sel) { return __builtin_amdgcn_perm(src, src, sel); }
__device__ __forceinline__ v4u perm4(unsigned src, const unsigned (&s)[4]) { return v4u{perm(src, s[0]), perm(src, s[1]), perm(src, s[2]), perm(src, s[3])}; }

struct __attribute__((aligned(16))) Lds {
  f32x4 df[4][64];
  f32x4 acc[4][32];
  float ml[8];
  unsigned short xn[2][2][80];
  unsigned short qh[2][128];
  unsigned tag; unsigned pad[3];
};

struct XRegs { v4u c[3]; };
__device__ __forceinline__ void x_issue(XRegs& xr, rsrc_t rx, int lane) {
  const int vo = (lane >> 5) * 1280 + (lane & 31) * 16;
  xr.c[0] = __builtin_amdgcn_raw_buffer_load_b128(rx, vo, 0, 0);
  xr.c[1] = __builtin_amdgcn_raw_buffer_load_b128(rx, vo, 512, 0);
  xr.c[2] = __builtin_amdgcn_raw_buffer_load_b128(rx, (lane & 16) ? OOB : vo, 1024, 0);
}
__device__ __forceinline__ void ln_slice(const XRegs& xr, int lane, int i, unsigned short* dst) {
  float sa = 0.f, sb = 0.f, sc = 0.f;
#pragma unroll
  for (int k = 0; k < 4; ++k) { sa = dot2(xr.c[0][k], 0x3f803f80u, sa); sb = dot2(xr.c[1][k], 0x3f803f80u, sb); sc = dot2(xr.c[2][k], 0x3f803f80u, sc); }
  const float mean = x16_sum(row_sum(sa + sb + sc)) * (1.0f / D);
  float q0 = 0.f, q1 = 0.f, q2 = 0.f, q3 = 0.f, q4 = 0.f, q5 = 0.f;
#pragma unroll
  for (int k = 0; k < 4; ++k) {
    const float a0 = bflo(xr.c[0][k]) - mean, a1 = bfhi(xr.c[0][k]) - mean, b0 = bflo(xr.c[1][k]) - mean, b1 = bfhi(xr.c[1][k]) - mean;
    const float c0 = bflo(xr.c[2][k]) - mean, c1 = bfhi(xr.c[2][k]) - mean;
    q0 = fmaf(a0, a0, q0); q1 = fmaf(a1, a1, q1); q2 = fmaf(b0, b0, q2); q3 = fmaf(b1, b1, q3); q4 = fmaf(c0, c0, q4); q5 = fmaf(c1, c1, q5);
  }
  float ss = (q0 + q1) + (q2 + q3) + ((lane & 16) ? 0.f : q4 + q5);
  ss = x16_sum(row_sum(ss));
  const float mb = rbf(mean), den = rbf(__builtin_amdgcn_sqrtf(rbf(rbf(ss * (1.0f / D)) + 1e-5f)));
  const float rden = __builtin_amdgcn_rcpf(den);
  const int rel = ((lane & 31) - 10 * i) & 31;
  const int cidx = 10 * i + rel;
  const v4u m = cidx < 32 ? xr.c[0] : cidx < 64 ? xr.c[1] : xr.c[2];
  v4u o;
#pragma unroll
  for (int k = 0; k < 4; ++k) { const unsigned d = cvtpk(bflo(m[k]) - mb, bfhi(m[k]) - mb); o[k] = cvtpk(bflo(d) * rden, bfhi(d) * rden); }
  if (rel < 10) *reinterpret_cast<v4u*>(dst + (lane >> 5) * 80 + rel * 8) = o;
  asm volatile("s_waitcnt lgkmcnt(0)" ::: "memory");
}

__global__ void __launch_bounds__(512, 2) __attribute__((aligned(4096)))
fused_decode(const unsigned short* __restrict__ x, const unsigned short* __restrict__ kc, const unsigned short* __restrict__ vc,
             const unsigned short* __restrict__ wq, const unsigned short* __restrict__ wo, const unsigned short* __restrict__ w1,
             const unsigned short* __restrict__ w2, unsigned short* __restrict__ out, unsigned char* __restrict__ ws) {
  __shared__ Lds L;
  const int tid = threadIdx.x, lane = tid & 63, wave = __builtin_amdgcn_readfirstlane(tid >> 6);
  const int bid = blockIdx.x, xcd = bid & 7, sl = bid >> 3, kq = sl >> 3, h = xcd * 4 + kq, i = sl & 7, b = i >> 2, j = i & 3;
  const int gi = kq * 8 + xcd;
  const bool ffn = gi < NG;
  const int g = lane >> 4, n = lane & 15;
  const rsrc_t rws = mkrsrc(ws, WS_END);
  asm volatile("" :: "s"(x), "s"(kc), "s"(vc), "s"(wq), "s"(wo), "s"(w1), "s"(w2), "s"(out), "s"(ws));
  const size_t kvbase = ((size_t)(b * H + h) * SK + (SK - WIN) + j * 512) * DH;
  const rsrc_t rk = mkrsrc(kc + kvbase, 131072);
  const int lo = wave * 1024 + lane * 16;
  v4u K[32];
  const int a = wave - 4;
  const bool st1 = a == 0 || (a == 1 && ffn);
  unsigned ep = 0; XRegs xr; v4u W[20];
  if (st1) {
    __builtin_amdgcn_s_setprio(3);
    const rsrc_t rx = mkrsrc(x, 2 * D * 2);
    x_issue(xr, rx, lane);
    ep = __builtin_amdgcn_raw_buffer_load_b32(rws, WS_EPOCH, 0, AUX_SC1);
    if (a == 0) {
      const rsrc_t rw = mkrsrc(wq, D * H * DH * 2);
      const int vo = (80 * i + 20 * g) * 8192 + h * 256 + n * 16;
#pragma unroll
      for (int t = 0; t < 20; ++t) W[t] = ld_nt(rw, vo, t * 8192);
    } else {
      const rsrc_t rw = mkrsrc(w1, D * DFF * 2);
      const int vo = (80 * i + 20 * g + (n >> 3)) * (DFF * 2) + gi * 128 + (n & 7) * 16;
#pragma unroll
      for (int u = 0; u < 10; ++u) W[u] = ld_nt(rw, vo, u * (DFF * 4));
    }
    SB();
  } else if (wave < 4) {
#pragma unroll
    for (int t = 0; t < KPRE; ++t) K[t] = ld_nt(rk, lo, t * 4096);
  }
  int wave2 = wave;
  asm volatile("" : "+s"(wave2));
  SB();
  if (__builtin_expect(wave2 < 4, 1)) {
    const rsrc_t rv = mkrsrc(vc + kvbase, 131072);
    v4u V[32];
    __builtin_amdgcn_s_sleep(KDELAY);
#pragma unroll
    for (int t = KPRE; t < 32; ++t) { if (t >= KWIN) VMCNT(KWIN - 1); K[t] = ld_nt(rk, lo, t * 4096); SB(); }
#pragma unroll
    for (int t = 0; t < VPRE; ++t) { VMCNT(KWIN - 1); V[t] = ld_nt(rv, lo, t * 4096); SB(); }
    unsigned sel[4];
#pragma unroll
    for (int jj = 0; jj < 4; ++jj) sel[jj] = psel(n & 7, n >> 3, jj);
    wg_barrier();
#pragma unroll
    for (int t = VPRE; t < VRING; ++t) { V[t] = ld_nt(rv, lo, t * 4096); SB(); }
    const v4u qv = *reinterpret_cast<const v4u*>(&L.qh[0][n * 8]);
    const unsigned tag = L.tag & 0xffu;
    float xs[32];
#pragma unroll
    for (int t = 0; t < 32; ++t) { float d = dot2(K[t].x, qv.x, 0.f); d = dot2(K[t].y, qv.y, d); d = dot2(K[t].z, qv.z, d); xs[t] = dot2(K[t].w, qv.w, d); }
#define TR_STEP(HALF, CTRL, BIT) _Pragma("unroll") for (int u = 0; u < (HALF); ++u) { \
      const float lo_ = xs[u], hi_ = xs[u + (HALF)]; const float t1_ = lo_ + dppf<CTRL>(lo_), t2_ = hi_ + dppf<CTRL>(hi_); xs[u] = (n & (BIT)) ? t2_ : t1_; }
    TR_STEP(16, ROR8, 8) TR_STEP(8, HMIR, 4) TR_STEP(4, XOR2, 2) TR_STEP(2, XOR1, 1)
#undef TR_STEP
    const float s0 = rbf(rbf(xs[0]) * 0.08838834764831845f), s1 = rbf(rbf(xs[1]) * 0.08838834764831845f);
    const float m = x32_max(x16_max(row_max(fmaxf(s0, s1))));
    const float p0 = __builtin_amdgcn_exp2f((s0 - m) * LOG2E), p1 = __builtin_amdgcn_exp2f((s1 - m) * LOG2E);
    const unsigned P0 = cvtpk(p0, p0 - rbf(p0)), P1 = cvtpk(p1, p1 - rbf(p1));
    const float lsum = x32_sum(x16_sum(row_sum(p0 + p1)));
    if (lane == 0) { L.ml[wave] = m; L.ml[4 + wave] = lsum; }
    f32x4 acc = {0.f, 0.f, 0.f, 0.f}, fw = {0.f, 0.f, 0.f, 0.f};
    float M = 0.f, Ls = 0.f;
#define PV(t) { const unsigned bc = dppu<NEWB + ((t) >> 1)>(((t) & 1) ? P1 : P0); acc = mfma32(V[t], perm4(bc, sel), acc); \
      if ((t) + VRING < 32) V[((t) + VRING) & 31] = ld_nt(rv, lo, ((t) + VRING) * 4096); SB(); \
      if ((t) + VRING == 31) { wg_barrier();     \
        const f32x4 mm = *reinterpret_cast<const f32x4*>(&L.ml[0]), ll = *reinterpret_cast<const f32x4*>(&L.ml[4]); \
        M = fmaxf(fmaxf(mm[0], mm[1]), fmaxf(mm[2], mm[3])); \
        _Pragma("unroll") for (int w = 0; w < 4; ++w) { fw[w] = __builtin_amdgcn_exp2f((mm[w] - M) * LOG2E); Ls = fmaf(fw[w], ll[w], Ls); } \
        asm volatile("" : "+v"(M), "+v"(Ls), "+v"(fw)); SB(); } }
    PV(0) PV(1) PV(2) PV(3) PV(4) PV(5) PV(6) PV(7) PV(8) PV(9) PV(10) PV(11) PV(12) PV(13) PV(14) PV(15)
    PV(16) PV(17) PV(18) PV(19) PV(20) PV(21) PV(22) PV(23) PV(24) PV(25) PV(26) PV(27) PV(28) PV(29) PV(30) PV(31)
#undef PV
#pragma unroll
    for (int k = 0; k < 4; ++k) acc[k] += dppf<ROR8>(acc[k]);
    if (n < 8) L.acc[wave][g * 8 + n] = acc;
    wg_barrier();
    if (wave == 0) {
      f32x4 av[4];
#pragma unroll
      for (int w = 0; w < 4; ++w) av[w] = L.acc[w][lane & 31];
      f32x4 v = (fw[0] * av[0] + fw[1] * av[1]) + (fw[2] * av[2] + fw[3] * av[3]);
      if (lane == 32) v = f32x4{M, Ls, M, Ls};
      if (lane <= 32) st_unit(rws, WS_AP + (h * 8 + i) * 640 + lane * 16, v, tag);
    }
    wg_barrier();
    if (wave == 1 && lane < 48 && ffn) {
      const f32x4* src = &L.df[0][lane];
      st_unit(rws, WS_FP + (i * 32 + gi) * 768 + lane * 16, (src[0] + src[64]) + (src[128] + src[192]), tag);
    }
  } else {
    __builtin_amdgcn_s_setprio(3);
    unsigned selE[4], selO[4];
#pragma unroll
    for (int jj = 0; jj < 4; ++jj) { selE[jj] = psel(n & 7, 0, jj); selO[jj] = psel(n & 7, 1, jj); }
    if (st1) {
      const unsigned tagw = ep + 1u, tag = tagw & 0xffu;
      ln_slice(xr, lane, i, &L.xn[a][0][0]);
      v2u xw[5];
#pragma unroll
      for (int u = 0; u < 5; ++u) xw[u] = *reinterpret_cast<const v2u*>(&L.xn[a][n >> 3][20 * g + 4 * u]);
      f32x4 d0 = {0.f, 0.f, 0.f, 0.f}, d1 = {0.f, 0.f, 0.f, 0.f};
      if (a == 0) {
#pragma unroll
        for (int u = 0; u < 10; ++u) { const unsigned src = (u & 1) ? xw[u >> 1].y : xw[u >> 1].x; d0 = mfma32(W[2 * u], perm4(src, selE), d0); d1 = mfma32(W[2 * u + 1], perm4(src, selO), d1); }
        d0 += d1;
      } else {
#pragma unroll
        for (int u = 0; u < 10; ++u) { const unsigned src = (u & 1) ? xw[u >> 1].y : xw[u >> 1].x; d0 = mfma32(W[u], perm4(src, selE), d0); d1 = mfma32(W[u], perm4(src, selO), d1); }
#pragma unroll
        for (int k = 0; k < 4; ++k) d0[k] = x32_sum(g < 2 ? d0[k] : d1[k]);
      }
      const int po = a ? WS_HP + (gi * 8 + i) * 512 + (n >> 3) * 256 : WS_QP + (h * 8 + i) * 1024 + (n >> 3) * 512;
      if (!a || g < 2) st_unit(rws, po + (g * 8 + (n & 7)) * 16, d0, tag);
      __builtin_amdgcn_s_sleep(GATHER_SLEEP);
      const int gs = a ? 512 : 1024;
      const int go = (a ? WS_HP + gi * 4096 : WS_QP + h * 8192 + b * 512) + (lane >> 5) * gs + (lane & 31) * 16;
      v4u G[4];
      for (unsigned spin = 0;; ++spin) {
        MEMBAR();
#pragma unroll
        for (int p = 0; p < 4; ++p) G[p] = ld_sc1(rws, go, p * 2 * gs);
        const unsigned bad = unit_bad(G[0], tag) | unit_bad(G[1], tag) | unit_bad(G[2], tag) | unit_bad(G[3], tag);
        if (__all((bad & 0xffu) == 0u) || spin > SPIN_CAP) break;
        __builtin_amdgcn_s_sleep(8);
      }
      f32x4 qv = (unit_val(G[0]) + unit_val(G[1])) + (unit_val(G[2]) + unit_val(G[3]));
#pragma unroll
      for (int k = 0; k < 4; ++k) { qv[k] = x32_sum(qv[k]); if (a) qv[k] = fmaxf(qv[k], 0.f); }
      const unsigned q01 = cvtpk(qv[0], qv[1]), q23 = cvtpk(qv[2], qv[3]);
      if (lane < 32) {
        unsigned short* dst = &L.qh[a][(lane >> 3) * 32 + (lane & 7)];
        dst[0] = (unsigned short)q01; dst[8] = (unsigned short)(q01 >> 16); dst[16] = (unsigned short)q23; dst[24] = (unsigned short)(q23 >> 16);
      }
      if (tid == 256) L.tag = tagw;
    }
    wg_barrier();
    const unsigned tagw = L.tag, tag = tagw & 0xffu;
    const v2u hw = *reinterpret_cast<const v2u*>(&L.qh[1][(n >> 3) * 64 + 16 * a + 4 * g]);
    const int ocol = 8 * (4 * (gi >> 3) + ((lane >> 1) & 3)) + 2 * (gi & 3) + (lane & 1);
    const int oidx = ((gi >> 2) & 1) * D + i * 80 + ocol;
    const bool ow_ = a == 0 && gi < 24 && lane < 8 && ocol < 80;
    unsigned xres = 0;
    if (ow_) xres = x[oidx];
    if (a == 2 && lane < 40) st_unit(rws, WS_AP + (h * 8 + i) * 640 + lane * 16, f32x4{0.f, 0.f, 0.f, 0.f}, tag ^ 0x80u);
    const rsrc_t r2 = mkrsrc(w2, DFF * D * 2), ro = mkrsrc(wo, H * DH * D * 2);
    v4u W2[4], WO[8];
    const int v2o = (n < 10 && ffn) ? (64 * gi + 16 * a + 4 * g) * 1280 + i * 160 + n * 16 : OOB;
    const int voo = n < 10 ? (128 * h + 32 * a + 2 * g) * 1280 + i * 160 + n * 16 : OOB;
    VMCNT(0);
    wg_barrier();
#pragma unroll
    for (int t = 0; t < 4; ++t) W2[t] = ld_nt(r2, v2o, t * 1280);
#pragma unroll
    for (int t = 0; t < 8; ++t) WO[t] = ld_nt(ro, voo, (8 * (t & 3) + (t >> 2)) * 1280);
    SB();
    wg_barrier();
    f32x4 df = {0.f, 0.f, 0.f, 0.f};
    df = mfma32(W2[0], perm4(hw.x, selE), df); df = mfma32(W2[1], perm4(hw.x, selO), df);
    df = mfma32(W2[2], perm4(hw.y, selE), df); df = mfma32(W2[3], perm4(hw.y, selO), df);
    L.df[a][lane] = df;
    wg_barrier();
    const int apo = WS_AP + (h * 8 + (n >> 3) * 4 + (n & 3)) * 640;
    const int auo = apo + (8 * a + 2 * g + ((n >> 2) & 1)) * 16;
    v4u AU = {0, 0, 0, 0}, AM = {0, 0, 0, 0};
    __builtin_amdgcn_s_sleep(P1FIRST);
    AU = __builtin_amdgcn_raw_buffer_load_b128(rws, auo, 0, AUX_SYS); AM = __builtin_amdgcn_raw_buffer_load_b128(rws, apo, 512, AUX_SYS);
    SB();
    {
      bool nu = ((unit_bad(AU, tag) | unit_bad(AM, tag)) & 0xffu) != 0u;
      for (unsigned spin = 0; __any(nu) && spin <= SPIN_CAP; ++spin) {
        __builtin_amdgcn_s_sleep(P1SLEEP);
        MEMBAR();
        const v4u u_ = __builtin_amdgcn_raw_buffer_load_b128(rws, nu ? auo : OOB, 0, AUX_SYS), m_ = __builtin_amdgcn_raw_buffer_load_b128(rws, nu ? apo : OOB, 512, AUX_SYS);
        if (nu) { AU = u_; AM = m_; }
        nu = nu && ((unit_bad(u_, tag) | unit_bad(m_, tag)) & 0xffu) != 0u;
      }
    }
    {
      const f32x4 am = unit_val(AM);
      float M = am[0]; M = fmaxf(M, dppf<XOR1>(M)); M = fmaxf(M, dppf<XOR2>(M));
      const float w = __builtin_amdgcn_exp2f((am[0] - M) * LOG2E);
      float Ls = w * am[1]; Ls += dppf<XOR1>(Ls); Ls += dppf<XOR2>(Ls);
      f32x4 o = unit_val(AU) * (w * __builtin_amdgcn_rcpf(Ls));
#pragma unroll
      for (int k = 0; k < 4; ++k) o[k] += dppf<XOR1>(o[k]);
#pragma unroll
      for (int k = 0; k < 4; ++k) o[k] += dppf<XOR2>(o[k]);
      const unsigned m01 = cvtpk(o[0], o[1]), m23 = cvtpk(o[2], o[3]), t01 = dppu<HMIR>(m01), t23 = dppu<HMIR>(m23);
      const bool hf = (n & 4) != 0;
      const unsigned ow[4] = {hf ? t01 : m01, hf ? t23 : m23, hf ? m01 : t01, hf ? m23 : t23};
      f32x4 ds = {0.f, 0.f, 0.f, 0.f}, dt = {0.f, 0.f, 0.f, 0.f};
#pragma unroll
      for (int t = 0; t < 4; ++t) { ds = mfma32(WO[2 * t], perm4(ow[t], selE), ds); dt = mfma32(WO[2 * t + 1], perm4(ow[t], selO), dt); }
      if (lane < 48) st_unit(rws, WS_SP + ((i * 32 + h) * 24 + (lane >> 1)) * 128 + a * 32 + (lane & 1) * 16, ds + dt, tag);
    }
    if (a == 0 && gi < 24) {
      const int p = lane >> 1;
      const int so = WS_SP + ((i * 32 + (lane >> 3)) * 24 + gi) * 128 + (lane & 7) * 16;
      const int fo = p < NG ? WS_FP + (i * 32 + p) * 768 + gi * 32 + (lane & 1) * 16 : OOB;
      v4u S[4], F0;
      __builtin_amdgcn_s_sleep(P2FIRST);
      for (unsigned spin = 0;; ++spin) {
        MEMBAR();
#pragma unroll
        for (int q = 0; q < 4; ++q) S[q] = __builtin_amdgcn_raw_buffer_load_b128(rws, so, q * (8 * 24 * 128), AUX_SYS);
        F0 = __builtin_amdgcn_raw_buffer_load_b128(rws, fo, 0, AUX_SYS);
        const unsigned bad = unit_bad(S[0], tag) | unit_bad(S[1], tag) | unit_bad(S[2], tag) | unit_bad(S[3], tag) | (p < NG ? unit_bad(F0, tag) : 0u);
        if (__all((bad & 0xffu) == 0u) || spin > SPIN_CAP) break;
        __builtin_amdgcn_s_sleep(P2SLEEP);
      }
      const f32x4 rs = (unit_val(S[0]) + unit_val(S[1])) + (unit_val(S[2]) + unit_val(S[3])), rf = unit_val(F0);
      float s2[2], f2[2];
#pragma unroll
      for (int k = 0; k < 2; ++k) {
        const float a0 = rs[k] + dppf<ROR4>(rs[k]), a1 = rs[k + 2] + dppf<ROR4>(rs[k + 2]), b0 = rf[k] + dppf<ROR4>(rf[k]), b1 = rf[k + 2] + dppf<ROR4>(rf[k + 2]);
        const float a0r = a0 + dppf<ROR8>(a0), a1r = a1 + dppf<ROR8>(a1), b0r = b0 + dppf<ROR8>(b0), b1r = b1 + dppf<ROR8>(b1);
        s2[k] = (lane & 4) ? a1r : a0r; f2[k] = (lane & 4) ? b1r : b0r;
      }
      const float sa = s2[0] + dppf<XOR2>(s2[0]), sb = s2[1] + dppf<XOR2>(s2[1]), fa = f2[0] + dppf<XOR2>(f2[0]), fb = f2[1] + dppf<XOR2>(f2[1]);
      const float sv = rbf(x32_sum(x16_sum((lane & 2) ? sb : sa))), fv = rbf(x32_sum(x16_sum((lane & 2) ? fb : fa)));
      const float gv = rbf(fv * __builtin_amdgcn_rcpf(1.0f + __builtin_amdgcn_exp2f(-fv * LOG2E)));
      const float o = bflo(xres) + rbf(sv * gv);
      if (ow_) out[oidx] = (unsigned short)cvtpk(o, o);
      if (gi == 0 && i == 0 && lane == 0) __builtin_amdgcn_raw_buffer_store_b32(tagw, rws, WS_EPOCH, 0, AUX_SC1);
    }
  }
}
}

extern "C" void kernel_launch(void* const* d_in, const int* in_sizes, int n_in, void* d_out, int out_size, void* d_ws, size_t ws_size, hipStream_t stream) {
  fused_decode<<<256, 512, 0, stream>>>((const unsigned short*)d_in[0], (const unsigned short*)d_in[1], (const unsigned short*)d_in[2],
                                        (const unsigned short*)d_in[3], (const unsigned short*)d_in[4], (const unsigned short*)d_in[6],
                                        (const unsigned short*)d_in[7], (unsigned short*)d_out, (unsigned char*)d_ws);
}
```

```cpp
#include <hip/hip_runtime.h>
#include <hip/hip_bf16.h>
#include <stdint.h>

namespace {
typedef unsigned v4u __attribute__((ext_vector_type(4)));
typedef unsigned v2u __attribute__((ext_vector_type(2)));
typedef float f32x4 __attribute__((ext_vector_type(4)));
typedef __bf16 bf16x2 __attribute__((ext_vector_type(2)));
typedef __bf16 bf16x8 __attribute__((ext_vector_type(8)));
typedef __amdgpu_buffer_rsrc_t rsrc_t;

constexpr int D = 640, H = 32, SK = 8192, DH = 128, WIN = 2048, DFF = 1728, NG = 27;
constexpr int AUX_NT = 2, AUX_SC1 = 16, RSRC3 = 0x00020000;
constexpr int OOB = 0x7ffffff0;
constexpr int WS_EPOCH = 0;
constexpr int WS_QP = 4096;
constexpr int WS_HP = WS_QP + 32 * 8 * 1024;
constexpr int WS_AP = WS_HP + 32 * 8 * 512;
constexpr int WS_SP = WS_AP + 32 * 8 * 640;
constexpr int WS_FP = WS_SP + 8 * 32 * 24 * 128;
constexpr int WS_END = WS_FP + 8 * 32 * 768;
constexpr unsigned SPIN_CAP = 1u << 18;
constexpr float LOG2E = 1.4426950408889634f;
constexpr int KWIN = 12, KPRE = 2, KDELAY = 10, VPRE = 20, VRING = 22;
constexpr int GATHER_SLEEP = 40, P1FIRST = 16, P1SLEEP = 2, P2FIRST = 20, P2SLEEP = 2;
constexpr int AUX_SYS = 17;

#define SB() __builtin_amdgcn_sched_barrier(0)
#define MEMBAR() asm volatile("" ::: "memory")
#define VMCNT(n) asm volatile("s_waitcnt vmcnt(%0)" ::"n"(n) : "memory")
__device__ __forceinline__ void wg_barrier() { SB(); asm volatile("s_waitcnt lgkmcnt(0)\n\ts_barrier" ::: "memory"); SB(); }
__device__ __forceinline__ float bflo(unsigned u) { return __uint_as_float(u << 16); }
__device__ __forceinline__ float bfhi(unsigned u) { return __uint_as_float(u & 0xffff0000u); }
__device__ __forceinline__ unsigned cvtpk(float lo, float hi) { bf16x2 v = {(__bf16)lo, (__bf16)hi}; return __builtin_bit_cast(unsigned, v); }
__device__ __forceinline__ float rbf(float f) { return __uint_as_float(cvtpk(f, f) & 0xffff0000u); }
template <int CTRL> __device__ __forceinline__ float dppf(float x) {
  return __builtin_bit_cast(float, __builtin_amdgcn_mov_dpp(__builtin_bit_cast(int, x), CTRL, 0xf, 0xf, true));
}
template <int CTRL> __device__ __forceinline__ unsigned dppu(unsigned x) { return (unsigned)__builtin_amdgcn_mov_dpp((int)x, CTRL, 0xf, 0xf, true); }
constexpr int XOR1 = 0xB1, XOR2 = 0x4E, HMIR = 0x141, ROR8 = 0x128, ROR4 = 0x124, NEWB = 0x150;
__device__ __forceinline__ float x16_sum(float x) { auto s = __builtin_amdgcn_permlane16_swap(__float_as_uint(x), __float_as_uint(x), false, false); return __uint_as_float(s[0]) + __uint_as_float(s[1]); }
__device__ __forceinline__ float x32_sum(float x) { auto s = __builtin_amdgcn_permlane32_swap(__float_as_uint(x), __float_as_uint(x), false, false); return __uint_as_float(s[0]) + __uint_as_float(s[1]); }
__device__ __forceinline__ float x16_max(float x) { auto s = __builtin_amdgcn_permlane16_swap(__float_as_uint(x), __float_as_uint(x), false, false); return fmaxf(__uint_as_float(s[0]), __uint_as_float(s[1])); }
__device__ __forceinline__ float x32_max(float x) { auto s = __builtin_amdgcn_permlane32_swap(__float_as_uint(x), __float_as_uint(x), false, false); return fmaxf(__uint_as_float(s[0]), __uint_as_float(s[1])); }
__device__ __forceinline__ float row_sum(float x) { x += dppf<XOR1>(x); x += dppf<XOR2>(x); x += dppf<HMIR>(x); x += dppf<ROR8>(x); return x; }
__device__ __forceinline__ float row_max(float x) { x = fmaxf(x, dppf<XOR1>(x)); x = fmaxf(x, dppf<XOR2>(x)); x = fmaxf(x, dppf<HMIR>(x)); x = fmaxf(x, dppf<ROR8>(x)); return x; }
__device__ __forceinline__ float dot2(unsigned k, unsigned q, float acc) {
  return __builtin_amdgcn_fdot2_f32_bf16(__builtin_bit_cast(bf16x2, k), __builtin_bit_cast(bf16x2, q), acc, false);
}
__device__ __forceinline__ f32x4 mfma32(v4u a, v4u b, f32x4 c) {
  return __builtin_amdgcn_mfma_f32_16x16x32_bf16(__builtin_bit_cast(bf16x8, a), __builtin_bit_cast(bf16x8, b), c, 0, 0, 0);
}
__device__ __forceinline__ rsrc_t mkrsrc(const void* p, unsigned bytes) { return __builtin_amdgcn_make_buffer_rsrc((void*)p, 0, bytes, RSRC3); }
__device__ __forceinline__ v4u ld_nt(rsrc_t r, int voff, int soff) { return __builtin_amdgcn_raw_buffer_load_b128(r, voff, soff, AUX_NT); }
__device__ __forceinline__ v4u ld_sc1(rsrc_t r, int voff, int soff) { return __builtin_amdgcn_raw_buffer_load_b128(r, voff, soff, AUX_SC1); }
__device__ __forceinline__ void st_unit(rsrc_t r, int voff, f32x4 v, unsigned tag) {
  v4u u;
#pragma unroll
  for (int k = 0; k < 4; ++k) u[k] = (__float_as_uint(v[k]) & 0xffffff00u) | tag;
  __builtin_amdgcn_raw_buffer_store_b128(u, r, voff, 0, AUX_SC1);
}
__device__ __forceinline__ unsigned unit_bad(v4u g, unsigned tag) { return (g.x ^ tag) | (g.y ^ tag) | (g.z ^ tag) | (g.w ^ tag); }
__device__ __forceinline__ f32x4 unit_val(v4u g) {
  return f32x4{__uint_as_float(g.x & 0xffffff00u), __uint_as_float(g.y & 0xffffff00u), __uint_as_float(g.z & 0xffffff00u), __uint_as_float(g.w & 0xffffff00u)};
}
__device__ __forceinline__ unsigned psel(int e, int sh, int jj) {
  const unsigned pair = (unsigned)(2 * sh) | ((unsigned)(2 * sh + 1) << 8);
  const unsigned s = (e & 1) ? ((pair << 16) | 0x0c0cu) : (0x0c0c0000u | pair);
  return (e >> 1) == jj ? s : 0x0c0c0c0cu;
}
__device__ __forceinline__ unsigned perm(unsigned src, unsigned sel) { return __builtin_amdgcn_perm(src, src, sel); }
__device__ __forceinline__ v4u perm4(unsigned src, const unsigned (&s)[4]) { return v4u{perm(src, s[0]), perm(src, s[1]), perm(src, s[2]), perm(src, s[3])}; }

struct __attribute__((aligned(16))) Lds {
  f32x4 df[4][64];
  f32x4 acc[4][32];
  float ml[8];
  unsigned short xn[2][2][80];
  unsigned short qh[2][128];
  unsigned tag; unsigned pad[3];
};

struct XRegs { v4u c[3]; };
__device__ __forceinline__ void x_issue(XRegs& xr, rsrc_t rx, int lane) {
  const int vo = (lane >> 5) * 1280 + (lane & 31) * 16;
  xr.c[0] = __builtin_amdgcn_raw_buffer_load_b128(rx, vo, 0, 0);
  xr.c[1] = __builtin_amdgcn_raw_buffer_load_b128(rx, vo, 512, 0);
  xr.c[2] = __builtin_amdgcn_raw_buffer_load_b128(rx, (lane & 16) ? OOB : vo, 1024, 0);
}
__device__ __forceinline__ void ln_slice(const XRegs& xr, int lane, int i, unsigned short* dst) {
  float sa = 0.f, sb = 0.f, sc = 0.f;
#pragma unroll
  for (int k = 0; k < 4; ++k) { sa = dot2(xr.c[0][k], 0x3f803f80u, sa); sb = dot2(xr.c[1][k], 0x3f803f80u, sb); sc = dot2(xr.c[2][k], 0x3f803f80u, sc); }
  const float mean = x16_sum(row_sum(sa + sb + sc)) * (1.0f / D);
  float q0 = 0.f, q1 = 0.f, q2 = 0.f, q3 = 0.f, q4 = 0.f, q5 = 0.f;
#pragma unroll
  for (int k = 0; k < 4; ++k) {
    const float a0 = bflo(xr.c[0][k]) - mean, a1 = bfhi(xr.c[0][k]) - mean, b0 = bflo(xr.c[1][k]) - mean, b1 = bfhi(xr.c[1][k]) - mean;
    const float c0 = bflo(xr.c[2][k]) - mean, c1 = bfhi(xr.c[2][k]) - mean;
    q0 = fmaf(a0, a0, q0); q1 = fmaf(a1, a1, q1); q2 = fmaf(b0, b0, q2); q3 = fmaf(b1, b1, q3); q4 = fmaf(c0, c0, q4); q5 = fmaf(c1, c1, q5);
  }
  float ss = (q0 + q1) + (q2 + q3) + ((lane & 16) ? 0.f : q4 + q5);
  ss = x16_sum(row_sum(ss));
  const float mb = rbf(mean), den = rbf(__builtin_amdgcn_sqrtf(rbf(rbf(ss * (1.0f / D)) + 1e-5f)));
  const float rden = __builtin_amdgcn_rcpf(den);
  const int rel = ((lane & 31) - 10 * i) & 31;
  const int cidx = 10 * i + rel;
  const v4u m = cidx < 32 ? xr.c[0] : cidx < 64 ? xr.c[1] : xr.c[2];
  v4u o;
#pragma unroll
  for (int k = 0; k < 4; ++k) { const unsigned d = cvtpk(bflo(m[k]) - mb, bfhi(m[k]) - mb); o[k] = cvtpk(bflo(d) * rden, bfhi(d) * rden); }
  if (rel < 10) *reinterpret_cast<v4u*>(dst + (lane >> 5) * 80 + rel * 8) = o;
  asm volatile("s_waitcnt lgkmcnt(0)" ::: "memory");
}

__global__ void __launch_bounds__(512, 2) __attribute__((aligned(4096)))
fused_decode(const unsigned short* __restrict__ x, const unsigned short* __restrict__ kc, const unsigned short* __restrict__ vc,
             const unsigned short* __restrict__ wq, const unsigned short* __restrict__ wo, const unsigned short* __restrict__ w1,
             const unsigned short* __restrict__ w2, unsigned short* __restrict__ out, unsigned char* __restrict__ ws) {
  __shared__ Lds L;
  const int tid = threadIdx.x, lane = tid & 63, wave = __builtin_amdgcn_readfirstlane(tid >> 6);
  const int bid = blockIdx.x, xcd = bid & 7, sl = bid >> 3, kq = sl >> 3, h = xcd * 4 + kq, i = sl & 7, b = i >> 2, j = i & 3;
  const int gi = kq * 8 + xcd;
  const bool ffn = gi < NG;
  const int g = lane >> 4, n = lane & 15;
  const rsrc_t rws = mkrsrc(ws, WS_END);
  const unsigned t_start = (unsigned)__builtin_amdgcn_s_memrealtime();
  asm volatile("" :: "s"(x), "s"(kc), "s"(vc), "s"(wq), "s"(wo), "s"(w1), "s"(w2), "s"(out), "s"(ws));
  const size_t kvbase = ((size_t)(b * H + h) * SK + (SK - WIN) + j * 512) * DH;
  const rsrc_t rk = mkrsrc(kc + kvbase, 131072);
  const int lo = wave * 1024 + lane * 16;
  v4u K[32];
  const int a = wave - 4;
  const bool st1 = a == 0 || (a == 1 && ffn);
  unsigned ep = 0; XRegs xr; v4u W[20];
  if (st1) {
    __builtin_amdgcn_s_setprio(3);
    const rsrc_t rx = mkrsrc(x, 2 * D * 2);
    x_issue(xr, rx, lane);
    ep = __builtin_amdgcn_raw_buffer_load_b32(rws, WS_EPOCH, 0, AUX_SC1);
    if (a == 0) {
      const rsrc_t rw = mkrsrc(wq, D * H * DH * 2);
      const int vo = (80 * i + 20 * g) * 8192 + h * 256 + n * 16;
#pragma unroll
      for (int t = 0; t < 20; ++t) W[t] = ld_nt(rw, vo, t * 8192);
    } else {
      const rsrc_t rw = mkrsrc(w1, D * DFF * 2);
      const int vo = (80 * i + 20 * g + (n >> 3)) * (DFF * 2) + gi * 128 + (n & 7) * 16;
#pragma unroll
      for (int u = 0; u < 10; ++u) W[u] = ld_nt(rw, vo, u * (DFF * 4));
    }
    SB();
  } else if (wave < 4) {
#pragma unroll
    for (int t = 0; t < KPRE; ++t) K[t] = ld_nt(rk, lo, t * 4096);
  }
  int wave2 = wave;
  asm volatile("" : "+s"(wave2));
  SB();
  if (__builtin_expect(wave2 < 4, 1)) {
    const rsrc_t rv = mkrsrc(vc + kvbase, 131072);
    v4u V[32];
    __builtin_amdgcn_s_sleep(KDELAY);
#pragma unroll
    for (int t = KPRE; t < 32; ++t) { if (t >= KWIN) VMCNT(KWIN - 1); K[t] = ld_nt(rk, lo, t * 4096); SB(); }
#pragma unroll
    for (int t = 0; t < VPRE; ++t) { VMCNT(KWIN - 1); V[t] = ld_nt(rv, lo, t * 4096); SB(); }
    unsigned sel[4];
#pragma unroll
    for (int jj = 0; jj < 4; ++jj) sel[jj] = psel(n & 7, n >> 3, jj);
    wg_barrier();
#pragma unroll
    for (int t = VPRE; t < VRING; ++t) { V[t] = ld_nt(rv, lo, t * 4096); SB(); }
    const v4u qv = *reinterpret_cast<const v4u*>(&L.qh[0][n * 8]);
    const unsigned tag = L.tag & 0xffu;
    float xs[32];
#pragma unroll
    for (int t = 0; t < 32; ++t) { float d = dot2(K[t].x, qv.x, 0.f); d = dot2(K[t].y, qv.y, d); d = dot2(K[t].z, qv.z, d); xs[t] = dot2(K[t].w, qv.w, d); }
#define TR_STEP(HALF, CTRL, BIT) _Pragma("unroll") for (int u = 0; u < (HALF); ++u) { \
      const float lo_ = xs[u], hi_ = xs[u + (HALF)]; const float t1_ = lo_ + dppf<CTRL>(lo_), t2_ = hi_ + dppf<CTRL>(hi_); xs[u] = (n & (BIT)) ? t2_ : t1_; }
    TR_STEP(16, ROR8, 8) TR_STEP(8, HMIR, 4) TR_STEP(4, XOR2, 2) TR_STEP(2, XOR1, 1)
#undef TR_STEP
    const float s0 = rbf(rbf(xs[0]) * 0.08838834764831845f), s1 = rbf(rbf(xs[1]) * 0.08838834764831845f);
    const float m = x32_max(x16_max(row_max(fmaxf(s0, s1))));
    const float p0 = __builtin_amdgcn_exp2f((s0 - m) * LOG2E), p1 = __builtin_amdgcn_exp2f((s1 - m) * LOG2E);
    const unsigned P0 = cvtpk(p0, p0 - rbf(p0)), P1 = cvtpk(p1, p1 - rbf(p1));
    const float lsum = x32_sum(x16_sum(row_sum(p0 + p1)));
    if (lane == 0) { L.ml[wave] = m; L.ml[4 + wave] = lsum; }
    f32x4 acc = {0.f, 0.f, 0.f, 0.f}, fw = {0.f, 0.f, 0.f, 0.f};
    float M = 0.f, Ls = 0.f;
#define PV(t) { const unsigned bc = dppu<NEWB + ((t) >> 1)>(((t) & 1) ? P1 : P0); acc = mfma32(V[t], perm4(bc, sel), acc); \
      if ((t) + VRING < 32) V[((t) + VRING) & 31] = ld_nt(rv, lo, ((t) + VRING) * 4096); SB(); \
      if ((t) + VRING == 31) { wg_barrier();     \
        const f32x4 mm = *reinterpret_cast<const f32x4*>(&L.ml[0]), ll = *reinterpret_cast<const f32x4*>(&L.ml[4]); \
        M = fmaxf(fmaxf(mm[0], mm[1]), fmaxf(mm[2], mm[3])); \
        _Pragma("unroll") for (int w = 0; w < 4; ++w) { fw[w] = __builtin_amdgcn_exp2f((mm[w] - M) * LOG2E); Ls = fmaf(fw[w], ll[w], Ls); } \
        asm volatile("" : "+v"(M), "+v"(Ls), "+v"(fw)); SB(); } }
    PV(0) PV(1) PV(2) PV(3) PV(4) PV(5) PV(6) PV(7) PV(8) PV(9) PV(10) PV(11) PV(12) PV(13) PV(14) PV(15)
    PV(16) PV(17) PV(18) PV(19) PV(20) PV(21) PV(22) PV(23) PV(24) PV(25) PV(26) PV(27) PV(28) PV(29) PV(30) PV(31)
#undef PV
#pragma unroll
    for (int k = 0; k < 4; ++k) acc[k] += dppf<ROR8>(acc[k]);
    if (n < 8) L.acc[wave][g * 8 + n] = acc;
    wg_barrier();
    if (wave == 0) {
      f32x4 av[4];
#pragma unroll
      for (int w = 0; w < 4; ++w) av[w] = L.acc[w][lane & 31];
      f32x4 v = (fw[0] * av[0] + fw[1] * av[1]) + (fw[2] * av[2] + fw[3] * av[3]);
      if (lane == 32) v = f32x4{M, Ls, M, Ls};
      if (lane <= 32) st_unit(rws, WS_AP + (h * 8 + i) * 640 + lane * 16, v, tag);
    }
    wg_barrier();
    if (wave == 1 && lane < 48 && ffn) {
      const f32x4* src = &L.df[0][lane];
      st_unit(rws, WS_FP + (i * 32 + gi) * 768 + lane * 16, (src[0] + src[64]) + (src[128] + src[192]), tag);
    }
  } else {
    __builtin_amdgcn_s_setprio(3);
    unsigned selE[4], selO[4];
#pragma unroll
    for (int jj = 0; jj < 4; ++jj) { selE[jj] = psel(n & 7, 0, jj); selO[jj] = psel(n & 7, 1, jj); }
    if (st1) {
      const unsigned tagw = ep + 1u, tag = tagw & 0xffu;
      ln_slice(xr, lane, i, &L.xn[a][0][0]);
      v2u xw[5];
#pragma unroll
      for (int u = 0; u < 5; ++u) xw[u] = *reinterpret_cast<const v2u*>(&L.xn[a][n >> 3][20 * g + 4 * u]);
      f32x4 d0 = {0.f, 0.f, 0.f, 0.f}, d1 = {0.f, 0.f, 0.f, 0.f};
      if (a == 0) {
#pragma unroll
        for (int u = 0; u < 10; ++u) { const unsigned src = (u & 1) ? xw[u >> 1].y : xw[u >> 1].x; d0 = mfma32(W[2 * u], perm4(src, selE), d0); d1 = mfma32(W[2 * u + 1], perm4(src, selO), d1); }
        d0 += d1;
      } else {
#pragma unroll
        for (int u = 0; u < 10; ++u) { const unsigned src = (u & 1) ? xw[u >> 1].y : xw[u >> 1].x; d0 = mfma32(W[u], perm4(src, selE), d0); d1 = mfma32(W[u], perm4(src, selO), d1); }
#pragma unroll
        for (int k = 0; k < 4; ++k) d0[k] = x32_sum(g < 2 ? d0[k] : d1[k]);
      }
      const int po = a ? WS_HP + (gi * 8 + i) * 512 + (n >> 3) * 256 : WS_QP + (h * 8 + i) * 1024 + (n >> 3) * 512;
      if (!a || g < 2) st_unit(rws, po + (g * 8 + (n & 7)) * 16, d0, tag);
      __builtin_amdgcn_s_sleep(GATHER_SLEEP);
      const int gs = a ? 512 : 1024;
      const int go = (a ? WS_HP + gi * 4096 : WS_QP + h * 8192 + b * 512) + (lane >> 5) * gs + (lane & 31) * 16;
      v4u G[4];
      for (unsigned spin = 0;; ++spin) {
        MEMBAR();
#pragma unroll
        for (int p = 0; p < 4; ++p) G[p] = ld_sc1(rws, go, p * 2 * gs);
        const unsigned bad = unit_bad(G[0], tag) | unit_bad(G[1], tag) | unit_bad(G[2], tag) | unit_bad(G[3], tag);
        if (__all((bad & 0xffu) == 0u) || spin > SPIN_CAP) break;
        __builtin_amdgcn_s_sleep(8);
      }
      f32x4 qv = (unit_val(G[0]) + unit_val(G[1])) + (unit_val(G[2]) + unit_val(G[3]));
#pragma unroll
      for (int k = 0; k < 4; ++k) { qv[k] = x32_sum(qv[k]); if (a) qv[k] = fmaxf(qv[k], 0.f); }
      const unsigned q01 = cvtpk(qv[0], qv[1]), q23 = cvtpk(qv[2], qv[3]);
      if (lane < 32) {
        unsigned short* dst = &L.qh[a][(lane >> 3) * 32 + (lane & 7)];
        dst[0] = (unsigned short)q01; dst[8] = (unsigned short)(q01 >> 16); dst[16] = (unsigned short)q23; dst[24] = (unsigned short)(q23 >> 16);
      }
      if (tid == 256) L.tag = tagw;
    }
    wg_barrier();
    const unsigned tagw = L.tag, tag = tagw & 0xffu;
    const v2u hw = *reinterpret_cast<const v2u*>(&L.qh[1][(n >> 3) * 64 + 16 * a + 4 * g]);
    const int ocol = 8 * (4 * (gi >> 3) + ((lane >> 1) & 3)) + 2 * (gi & 3) + (lane & 1);
    const int oidx = ((gi >> 2) & 1) * D + i * 80 + ocol;
    const bool ow_ = a == 0 && gi < 24 && lane < 8 && ocol < 80;
    unsigned xres = 0;
    if (ow_) xres = x[oidx];
    if (a == 2 && lane < 40) st_unit(rws, WS_AP + (h * 8 + i) * 640 + lane * 16, f32x4{0.f, 0.f, 0.f, 0.f}, tag ^ 0x80u);
    const rsrc_t r2 = mkrsrc(w2, DFF * D * 2), ro = mkrsrc(wo, H * DH * D * 2);
    v4u W2[4], WO[8];
    const int v2o = (n < 10 && ffn) ? (64 * gi + 16 * a + 4 * g) * 1280 + i * 160 + n * 16 : OOB;
    const int voo = n < 10 ? (128 * h + 32 * a + 2 * g) * 1280 + i * 160 + n * 16 : OOB;
    VMCNT(0);
    wg_barrier();
#pragma unroll
    for (int t = 0; t < 4; ++t) W2[t] = ld_nt(r2, v2o, t * 1280);
#pragma unroll
    for (int t = 0; t < 8; ++t) WO[t] = ld_nt(ro, voo, (8 * (t & 3) + (t >> 2)) * 1280);
    SB();
    wg_barrier();
    f32x4 df = {0.f, 0.f, 0.f, 0.f};
    df = mfma32(W2[0], perm4(hw.x, selE), df); df = mfma32(W2[1], perm4(hw.x, selO), df);
    df = mfma32(W2[2], perm4(hw.y, selE), df); df = mfma32(W2[3], perm4(hw.y, selO), df);
    L.df[a][lane] = df;
    wg_barrier();
    const int apo = WS_AP + (h * 8 + (n >> 3) * 4 + (n & 3)) * 640;
    const int auo = apo + (8 * a + 2 * g + ((n >> 2) & 1)) * 16;
    v4u AU = {0, 0, 0, 0}, AM = {0, 0, 0, 0};
    __builtin_amdgcn_s_sleep(P1FIRST);
    AU = __builtin_amdgcn_raw_buffer_load_b128(rws, auo, 0, AUX_SYS); AM = __builtin_amdgcn_raw_buffer_load_b128(rws, apo, 512, AUX_SYS);
    SB();
    {
      bool nu = ((unit_bad(AU, tag) | unit_bad(AM, tag)) & 0xffu) != 0u;
      for (unsigned spin = 0; __any(nu) && spin <= SPIN_CAP; ++spin) {
        __builtin_amdgcn_s_sleep(P1SLEEP);
        MEMBAR();
        const v4u u_ = __builtin_amdgcn_raw_buffer_load_b128(rws, nu ? auo : OOB, 0, AUX_SYS), m_ = __builtin_amdgcn_raw_buffer_load_b128(rws, nu ? apo : OOB, 512, AUX_SYS);
        if (nu) { AU = u_; AM = m_; }
        nu = nu && ((unit_bad(u_, tag) | unit_bad(m_, tag)) & 0xffu) != 0u;
      }
    }
    {
      const f32x4 am = unit_val(AM);
      float M = am[0]; M = fmaxf(M, dppf<XOR1>(M)); M = fmaxf(M, dppf<XOR2>(M));
      const float w = __builtin_amdgcn_exp2f((am[0] - M) * LOG2E);
      float Ls = w * am[1]; Ls += dppf<XOR1>(Ls); Ls += dppf<XOR2>(Ls);
      f32x4 o = unit_val(AU) * (w * __builtin_amdgcn_rcpf(Ls));
#pragma unroll
      for (int k = 0; k < 4; ++k) o[k] += dppf<XOR1>(o[k]);
#pragma unroll
      for (int k = 0; k < 4; ++k) o[k] += dppf<XOR2>(o[k]);
      const unsigned m01 = cvtpk(o[0], o[1]), m23 = cvtpk(o[2], o[3]), t01 = dppu<HMIR>(m01), t23 = dppu<HMIR>(m23);
      const bool hf = (n & 4) != 0;
      const unsigned ow[4] = {hf ? t01 : m01, hf ? t23 : m23, hf ? m01 : t01, hf ? m23 : t23};
      f32x4 ds = {0.f, 0.f, 0.f, 0.f}, dt = {0.f, 0.f, 0.f, 0.f};
#pragma unroll
      for (int t = 0; t < 4; ++t) { ds = mfma32(WO[2 * t], perm4(ow[t], selE), ds); dt = mfma32(WO[2 * t + 1], perm4(ow[t], selO), dt); }
      if (lane < 48) st_unit(rws, WS_SP + ((i * 32 + h) * 24 + (lane >> 1)) * 128 + a * 32 + (lane & 1) * 16, ds + dt, tag);
    }
    if (a == 0 && gi < 24) {
      const int p = lane >> 1;
      const int so = WS_SP + ((i * 32 + (lane >> 3)) * 24 + gi) * 128 + (lane & 7) * 16;
      const int fo = p < NG ? WS_FP + (i * 32 + p) * 768 + gi * 32 + (lane & 1) * 16 : OOB;
      v4u S[4], F0;
      __builtin_amdgcn_s_sleep(P2FIRST);
      for (unsigned spin = 0;; ++spin) {
        MEMBAR();
#pragma unroll
        for (int q = 0; q < 4; ++q) S[q] = __builtin_amdgcn_raw_buffer_load_b128(rws, so, q * (8 * 24 * 128), AUX_SYS);
        F0 = __builtin_amdgcn_raw_buffer_load_b128(rws, fo, 0, AUX_SYS);
        const unsigned bad = unit_bad(S[0], tag) | unit_bad(S[1], tag) | unit_bad(S[2], tag) | unit_bad(S[3], tag) | (p < NG ? unit_bad(F0, tag) : 0u);
        if (__all((bad & 0xffu) == 0u) || spin > SPIN_CAP) break;
        __builtin_amdgcn_s_sleep(P2SLEEP);
      }
      const unsigned t_fin = (unsigned)__builtin_amdgcn_s_memrealtime();
      const f32x4 rs = (unit_val(S[0]) + unit_val(S[1])) + (unit_val(S[2]) + unit_val(S[3])), rf = unit_val(F0);
      float s2[2], f2[2];
#pragma unroll
      for (int k = 0; k < 2; ++k) {
        const float a0 = rs[k] + dppf<ROR4>(rs[k]), a1 = rs[k + 2] + dppf<ROR4>(rs[k + 2]), b0 = rf[k] + dppf<ROR4>(rf[k]), b1 = rf[k + 2] + dppf<ROR4>(rf[k + 2]);
        const float a0r = a0 + dppf<ROR8>(a0), a1r = a1 + dppf<ROR8>(a1), b0r = b0 + dppf<ROR8>(b0), b1r = b1 + dppf<ROR8>(b1);
        s2[k] = (lane & 4) ? a1r : a0r; f2[k] = (lane & 4) ? b1r : b0r;
      }
      const float sa = s2[0] + dppf<XOR2>(s2[0]), sb = s2[1] + dppf<XOR2>(s2[1]), fa = f2[0] + dppf<XOR2>(f2[0]), fb = f2[1] + dppf<XOR2>(f2[1]);
      const float sv = rbf(x32_sum(x16_sum((lane & 2) ? sb : sa))), fv = rbf(x32_sum(x16_sum((lane & 2) ? fb : fa)));
      const float gv = rbf(fv * __builtin_amdgcn_rcpf(1.0f + __builtin_amdgcn_exp2f(-fv * LOG2E)));
      const float o = bflo(xres) + rbf(sv * gv);
      if (ow_) out[oidx] = (unsigned short)cvtpk(o, o);
      if (gi == 0 && i == 0 && lane == 0) __builtin_amdgcn_raw_buffer_store_b32(tagw, rws, WS_EPOCH, 0, 0);
      if (bid == 0) { const unsigned until = t_start + 4000u + (0u); while ((int)((unsigned)__builtin_amdgcn_s_memrealtime() - until) < 0) __builtin_amdgcn_s_sleep(1); }
    }
  }
}
}

extern "C" void kernel_launch(void* const* d_in, const int* in_sizes, int n_in, void* d_out, int out_size, void* d_ws, size_t ws_size, hipStream_t stream) {
  fused_decode<<<256, 512, 0, stream>>>((const unsigned short*)d_in[0], (const unsigned short*)d_in[1], (const unsigned short*)d_in[2],
                                        (const unsigned short*)d_in[3], (const unsigned short*)d_in[4], (const unsigned short*)d_in[6],
                                        (const unsigned short*)d_in[7], (unsigned short*)d_out, (unsigned char*)d_ws);
}
```

```cpp
#include <hip/hip_runtime.h>
#include <hip/hip_bf16.h>
#include <stdint.h>

namespace {
typedef unsigned v4u __attribute__((ext_vector_type(4)));
typedef unsigned v2u __attribute__((ext_vector_type(2)));
typedef float f32x4 __attribute__((ext_vector_type(4)));
typedef __bf16 bf16x2 __attribute__((ext_vector_type(2)));
typedef __bf16 bf16x8 __attribute__((ext_vector_type(8)));
typedef __amdgpu_buffer_rsrc_t rsrc_t;

constexpr int D = 640, H = 32, SK = 8192, DH = 128, WIN = 2048, DFF = 1728, NG = 27;
constexpr int AUX_NT = 2, AUX_SC1 = 16, RSRC3 = 0x00020000;
constexpr int OOB = 0x7ffffff0;
constexpr int WS_EPOCH = 0;
constexpr int WS_QP = 4096;
constexpr int WS_HP = WS_QP + 32 * 8 * 1024;
constexpr int WS_AP = WS_HP + 32 * 8 * 512;
constexpr int WS_SP = WS_AP + 32 * 8 * 640;
constexpr int WS_FP = WS_SP + 8 * 32 * 24 * 128;
constexpr int WS_END = WS_FP + 8 * 32 * 768;
constexpr unsigned SPIN_CAP = 1u << 18;
constexpr float LOG2E = 1.4426950408889634f;
constexpr int KWIN = 12, KPRE = 2, KDELAY = 10, VPRE = 20, VRING = 22;
constexpr int GATHER_SLEEP = 40, P1FIRST = 16, P1SLEEP = 2, P2FIRST = 20, P2SLEEP = 2;
constexpr unsigned PACE0 = 40, PACE = 15;
constexpr int AUX_SYS = 17;

#define SB() __builtin_amdgcn_sched_barrier(0)
#define MEMBAR() asm volatile("" ::: "memory")
#define VMCNT(n) asm volatile("s_waitcnt vmcnt(%0)" ::"n"(n) : "memory")
__device__ __forceinline__ void wg_barrier() { SB(); asm volatile("s_waitcnt lgkmcnt(0)\n\ts_barrier" ::: "memory"); SB(); }
__device__ __forceinline__ float bflo(unsigned u) { return __uint_as_float(u << 16); }
__device__ __forceinline__ float bfhi(unsigned u) { return __uint_as_float(u & 0xffff0000u); }
__device__ __forceinline__ unsigned cvtpk(float lo, float hi) { bf16x2 v = {(__bf16)lo, (__bf16)hi}; return __builtin_bit_cast(unsigned, v); }
__device__ __forceinline__ float rbf(float f) { return __uint_as_float(cvtpk(f, f) & 0xffff0000u); }
template <int CTRL> __device__ __forceinline__ float dppf(float x) {
  return __builtin_bit_cast(float, __builtin_amdgcn_mov_dpp(__builtin_bit_cast(int, x), CTRL, 0xf, 0xf, true));
}
template <int CTRL> __device__ __forceinline__ unsigned dppu(unsigned x) { return (unsigned)__builtin_amdgcn_mov_dpp((int)x, CTRL, 0xf, 0xf, true); }
constexpr int XOR1 = 0xB1, XOR2 = 0x4E, HMIR = 0x141, ROR8 = 0x128, ROR4 = 0x124, NEWB = 0x150;
__device__ __forceinline__ float x16_sum(float x) { auto s = __builtin_amdgcn_permlane16_swap(__float_as_uint(x), __float_as_uint(x), false, false); return __uint_as_float(s[0]) + __uint_as_float(s[1]); }
__device__ __forceinline__ float x32_sum(float x) { auto s = __builtin_amdgcn_permlane32_swap(__float_as_uint(x), __float_as_uint(x), false, false); return __uint_as_float(s[0]) + __uint_as_float(s[1]); }
__device__ __forceinline__ float x16_max(float x) { auto s = __builtin_amdgcn_permlane16_swap(__float_as_uint(x), __float_as_uint(x), false, false); return fmaxf(__uint_as_float(s[0]), __uint_as_float(s[1])); }
__device__ __forceinline__ float x32_max(float x) { auto s = __builtin_amdgcn_permlane32_swap(__float_as_uint(x), __float_as_uint(x), false, false); return fmaxf(__uint_as_float(s[0]), __uint_as_float(s[1])); }
__device__ __forceinline__ float row_sum(float x) { x += dppf<XOR1>(x); x += dppf<XOR2>(x); x += dppf<HMIR>(x); x += dppf<ROR8>(x); return x; }
__device__ __forceinline__ float row_max(float x) { x = fmaxf(x, dppf<XOR1>(x)); x = fmaxf(x, dppf<XOR2>(x)); x = fmaxf(x, dppf<HMIR>(x)); x = fmaxf(x, dppf<ROR8>(x)); return x; }
__device__ __forceinline__ float dot2(unsigned k, unsigned q, float acc) {
  return __builtin_amdgcn_fdot2_f32_bf16(__builtin_bit_cast(bf16x2, k), __builtin_bit_cast(bf16x2, q), acc, false);
}
__device__ __forceinline__ f32x4 mfma32(v4u a, v4u b, f32x4 c) {
  return __builtin_amdgcn_mfma_f32_16x16x32_bf16(__builtin_bit_cast(bf16x8, a), __builtin_bit_cast(bf16x8, b), c, 0, 0, 0);
}
__device__ __forceinline__ rsrc_t mkrsrc(const void* p, unsigned bytes) { return __builtin_amdgcn_make_buffer_rsrc((void*)p, 0, bytes, RSRC3); }
__device__ __forceinline__ v4u ld_nt(rsrc_t r, int voff, int soff) { return __builtin_amdgcn_raw_buffer_load_b128(r, voff, soff, AUX_NT); }
__device__ __forceinline__ v4u ld_sc1(rsrc_t r, int voff, int soff) { return __builtin_amdgcn_raw_buffer_load_b128(r, voff, soff, AUX_SC1); }
__device__ __forceinline__ void st_unit(rsrc_t r, int voff, f32x4 v, unsigned tag) {
  v4u u;
#pragma unroll
  for (int k = 0; k < 4; ++k) u[k] = (__float_as_uint(v[k]) & 0xffffff00u) | tag;
  __builtin_amdgcn_raw_buffer_store_b128(u, r, voff, 0, AUX_SC1);
}
__device__ __forceinline__ unsigned unit_bad(v4u g, unsigned tag) { return (g.x ^ tag) | (g.y ^ tag) | (g.z ^ tag) | (g.w ^ tag); }
__device__ __forceinline__ f32x4 unit_val(v4u g) {
  return f32x4{__uint_as_float(g.x & 0xffffff00u), __uint_as_float(g.y & 0xffffff00u), __uint_as_float(g.z & 0xffffff00u), __uint_as_float(g.w & 0xffffff00u)};
}
__device__ __forceinline__ unsigned psel(int e, int sh, int jj) {
  const unsigned pair = (unsigned)(2 * sh) | ((unsigned)(2 * sh + 1) << 8);
  const unsigned s = (e & 1) ? ((pair << 16) | 0x0c0cu) : (0x0c0c0000u | pair);
  return (e >> 1) == jj ? s : 0x0c0c0c0cu;
}
__device__ __forceinline__ unsigned perm(unsigned src, unsigned sel) { return __builtin_amdgcn_perm(src, src, sel); }
__device__ __forceinline__ v4u perm4(unsigned src, const unsigned (&s)[4]) { return v4u{perm(src, s[0]), perm(src, s[1]), perm(src, s[2]), perm(src, s[3])}; }

struct __attribute__((aligned(16))) Lds {
  f32x4 df[4][64];
  f32x4 acc[4][32];
  float ml[8];
  unsigned short xn[2][2][80];
  unsigned short qh[2][128];
  unsigned tag; unsigned pad[3];
};

struct XRegs { v4u c[3]; };
__device__ __forceinline__ void x_issue(XRegs& xr, rsrc_t rx, int lane) {
  const int vo = (lane >> 5) * 1280 + (lane & 31) * 16;
  xr.c[0] = __builtin_amdgcn_raw_buffer_load_b128(rx, vo, 0, 0);
  xr.c[1] = __builtin_amdgcn_raw_buffer_load_b128(rx, vo, 512, 0);
  xr.c[2] = __builtin_amdgcn_raw_buffer_load_b128(rx, (lane & 16) ? OOB : vo, 1024, 0);
}
__device__ __forceinline__ void ln_slice(const XRegs& xr, int lane, int i, unsigned short* dst) {
  float sa = 0.f, sb = 0.f, sc = 0.f;
#pragma unroll
  for (int k = 0; k < 4; ++k) { sa = dot2(xr.c[0][k], 0x3f803f80u, sa); sb = dot2(xr.c[1][k], 0x3f803f80u, sb); sc = dot2(xr.c[2][k], 0x3f803f80u, sc); }
  const float mean = x16_sum(row_sum(sa + sb + sc)) * (1.0f / D);
  float q0 = 0.f, q1 = 0.f, q2 = 0.f, q3 = 0.f, q4 = 0.f, q5 = 0.f;
#pragma unroll
  for (int k = 0; k < 4; ++k) {
    const float a0 = bflo(xr.c[0][k]) - mean, a1 = bfhi(xr.c[0][k]) - mean, b0 = bflo(xr.c[1][k]) - mean, b1 = bfhi(xr.c[1][k]) - mean;
    const float c0 = bflo(xr.c[2][k]) - mean, c1 = bfhi(xr.c[2][k]) - mean;
    q0 = fmaf(a0, a0, q0); q1 = fmaf(a1, a1, q1); q2 = fmaf(b0, b0, q2); q3 = fmaf(b1, b1, q3); q4 = fmaf(c0, c0, q4); q5 = fmaf(c1, c1, q5);
  }
  float ss = (q0 + q1) + (q2 + q3) + ((lane & 16) ? 0.f : q4 + q5);
  ss = x16_sum(row_sum(ss));
  const float mb = rbf(mean), den = rbf(__builtin_amdgcn_sqrtf(rbf(rbf(ss * (1.0f / D)) + 1e-5f)));
  const float rden = __builtin_amdgcn_rcpf(den);
  const int rel = ((lane & 31) - 10 * i) & 31;
  const int cidx = 10 * i + rel;
  const v4u m = cidx < 32 ? xr.c[0] : cidx < 64 ? xr.c[1] : xr.c[2];
  v4u o;
#pragma unroll
  for (int k = 0; k < 4; ++k) { const unsigned d = cvtpk(bflo(m[k]) - mb, bfhi(m[k]) - mb); o[k] = cvtpk(bflo(d) * rden, bfhi(d) * rden); }
  if (rel < 10) *reinterpret_cast<v4u*>(dst + (lane >> 5) * 80 + rel * 8) = o;
  asm volatile("s_waitcnt lgkmcnt(0)" ::: "memory");
}

__global__ void __launch_bounds__(512, 2) __attribute__((aligned(4096)))
fused_decode(const unsigned short* __restrict__ x, const unsigned short* __restrict__ kc, const unsigned short* __restrict__ vc,
             const unsigned short* __restrict__ wq, const unsigned short* __restrict__ wo, const unsigned short* __restrict__ w1,
             const unsigned short* __restrict__ w2, unsigned short* __restrict__ out, unsigned char* __restrict__ ws) {
  __shared__ Lds L;
  const int tid = threadIdx.x, lane = tid & 63, wave = __builtin_amdgcn_readfirstlane(tid >> 6);
  const int bid = blockIdx.x, xcd = bid & 7, sl = bid >> 3, kq = sl >> 3, h = xcd * 4 + kq, i = sl & 7, b = i >> 2, j = i & 3;
  const int gi = kq * 8 + xcd;
  const bool ffn = gi < NG;
  const int g = lane >> 4, n = lane & 15;
  const rsrc_t rws = mkrsrc(ws, WS_END);
  const unsigned t_start = (unsigned)__builtin_amdgcn_s_memrealtime();
  asm volatile("" :: "s"(x), "s"(kc), "s"(vc), "s"(wq), "s"(wo), "s"(w1), "s"(w2), "s"(out), "s"(ws));
  const size_t kvbase = ((size_t)(b * H + h) * SK + (SK - WIN) + j * 512) * DH;
  const rsrc_t rk = mkrsrc(kc + kvbase, 131072);
  const int lo = wave * 1024 + lane * 16;
  v4u K[32];
  const int a = wave - 4;
  const bool st1 = a == 0 || (a == 1 && ffn);
  unsigned ep = 0; XRegs xr; v4u W[20];
  if (st1) {
    __builtin_amdgcn_s_setprio(3);
    const rsrc_t rx = mkrsrc(x, 2 * D * 2);
    x_issue(xr, rx, lane);
    ep = __builtin_amdgcn_raw_buffer_load_b32(rws, WS_EPOCH, 0, AUX_SC1);
    if (a == 0) {
      const rsrc_t rw = mkrsrc(wq, D * H * DH * 2);
      const int vo = (80 * i + 20 * g) * 8192 + h * 256 + n * 16;
#pragma unroll
      for (int t = 0; t < 20; ++t) W[t] = ld_nt(rw, vo, t * 8192);
    } else {
      const rsrc_t rw = mkrsrc(w1, D * DFF * 2);
      const int vo = (80 * i + 20 * g + (n >> 3)) * (DFF * 2) + gi * 128 + (n & 7) * 16;
#pragma unroll
      for (int u = 0; u < 10; ++u) W[u] = ld_nt(rw, vo, u * (DFF * 4));
    }
    SB();
  } else if (wave < 4) {
#pragma unroll
    for (int t = 0; t < KPRE; ++t) K[t] = ld_nt(rk, lo, t * 4096);
  }
  int wave2 = wave;
  asm volatile("" : "+s"(wave2));
  SB();
  if (__builtin_expect(wave2 < 4, 1)) {
    const rsrc_t rv = mkrsrc(vc + kvbase, 131072);
    v4u V[32];
    __builtin_amdgcn_s_sleep(KDELAY);
#define PACE_WAIT(t) if (((t) & 3) == 0 && (t) >= KWIN) { const unsigned tgt_ = t_start + PACE0 + (t) * PACE; \
      while ((int)((unsigned)__builtin_amdgcn_s_memrealtime() - tgt_) < 0) __builtin_amdgcn_s_sleep(1); SB(); }
#pragma unroll
    for (int t = KPRE; t < 32; ++t) { if (t >= KWIN) VMCNT(KWIN - 1); PACE_WAIT(t) K[t] = ld_nt(rk, lo, t * 4096); SB(); }
#pragma unroll
    for (int t = 0; t < VPRE; ++t) { VMCNT(KWIN - 1); PACE_WAIT(32 + t) V[t] = ld_nt(rv, lo, t * 4096); SB(); }
    unsigned sel[4];
#pragma unroll
    for (int jj = 0; jj < 4; ++jj) sel[jj] = psel(n & 7, n >> 3, jj);
    wg_barrier();
#pragma unroll
    for (int t = VPRE; t < VRING; ++t) { V[t] = ld_nt(rv, lo, t * 4096); SB(); }
    const v4u qv = *reinterpret_cast<const v4u*>(&L.qh[0][n * 8]);
    const unsigned tag = L.tag & 0xffu;
    float xs[32];
#pragma unroll
    for (int t = 0; t < 32; ++t) { float d = dot2(K[t].x, qv.x, 0.f); d = dot2(K[t].y, qv.y, d); d = dot2(K[t].z, qv.z, d); xs[t] = dot2(K[t].w, qv.w, d); }
#define TR_STEP(HALF, CTRL, BIT) _Pragma("unroll") for (int u = 0; u < (HALF); ++u) { \
      const float lo_ = xs[u], hi_ = xs[u + (HALF)]; const float t1_ = lo_ + dppf<CTRL>(lo_), t2_ = hi_ + dppf<CTRL>(hi_); xs[u] = (n & (BIT)) ? t2_ : t1_; }
    TR_STEP(16, ROR8, 8) TR_STEP(8, HMIR, 4) TR_STEP(4, XOR2, 2) TR_STEP(2, XOR1, 1)
#undef TR_STEP
    const float s0 = rbf(rbf(xs[0]) * 0.08838834764831845f), s1 = rbf(rbf(xs[1]) * 0.08838834764831845f);
    const float m = x32_max(x16_max(row_max(fmaxf(s0, s1))));
    const float p0 = __builtin_amdgcn_exp2f((s0 - m) * LOG2E), p1 = __builtin_amdgcn_exp2f((s1 - m) * LOG2E);
    const unsigned P0 = cvtpk(p0, p0 - rbf(p0)), P1 = cvtpk(p1, p1 - rbf(p1));
    const float lsum = x32_sum(x16_sum(row_sum(p0 + p1)));
    if (lane == 0) { L.ml[wave] = m; L.ml[4 + wave] = lsum; }
    f32x4 acc = {0.f, 0.f, 0.f, 0.f}, fw = {0.f, 0.f, 0.f, 0.f};
    float M = 0.f, Ls = 0.f;
#define PV(t) { const unsigned bc = dppu<NEWB + ((t) >> 1)>(((t) & 1) ? P1 : P0); acc = mfma32(V[t], perm4(bc, sel), acc); \
      if ((t) + VRING < 32) { PACE_WAIT(32 + VRING + (t)) V[((t) + VRING) & 31] = ld_nt(rv, lo, ((t) + VRING) * 4096); } SB(); \
      if ((t) + VRING == 31) { wg_barrier();     \
        const f32x4 mm = *reinterpret_cast<const f32x4*>(&L.ml[0]), ll = *reinterpret_cast<const f32x4*>(&L.ml[4]); \
        M = fmaxf(fmaxf(mm[0], mm[1]), fmaxf(mm[2], mm[3])); \
        _Pragma("unroll") for (int w = 0; w < 4; ++w) { fw[w] = __builtin_amdgcn_exp2f((mm[w] - M) * LOG2E); Ls = fmaf(fw[w], ll[w], Ls); } \
        asm volatile("" : "+v"(M), "+v"(Ls), "+v"(fw)); SB(); } }
    PV(0) PV(1) PV(2) PV(3) PV(4) PV(5) PV(6) PV(7) PV(8) PV(9) PV(10) PV(11) PV(12) PV(13) PV(14) PV(15)
    PV(16) PV(17) PV(18) PV(19) PV(20) PV(21) PV(22) PV(23) PV(24) PV(25) PV(26) PV(27) PV(28) PV(29) PV(30) PV(31)
#undef PV
#undef PACE_WAIT
#pragma unroll
    for (int k = 0; k < 4; ++k) acc[k] += dppf<ROR8>(acc[k]);
    if (n < 8) L.acc[wave][g * 8 + n] = acc;
    wg_barrier();
    if (wave == 0) {
      f32x4 av[4];
#pragma unroll
      for (int w = 0; w < 4; ++w) av[w] = L.acc[w][lane & 31];
      f32x4 v = (fw[0] * av[0] + fw[1] * av[1]) + (fw[2] * av[2] + fw[3] * av[3]);
      if (lane == 32) v = f32x4{M, Ls, M, Ls};
      if (lane <= 32) st_unit(rws, WS_AP + (h * 8 + i) * 640 + lane * 16, v, tag);
    }
    wg_barrier();
    if (wave == 1 && lane < 48 && ffn) {
      const f32x4* src = &L.df[0][lane];
      st_unit(rws, WS_FP + (i * 32 + gi) * 768 + lane * 16, (src[0] + src[64]) + (src[128] + src[192]), tag);
    }
  } else {
    __builtin_amdgcn_s_setprio(3);
    unsigned selE[4], selO[4];
#pragma unroll
    for (int jj = 0; jj < 4; ++jj) { selE[jj] = psel(n & 7, 0, jj); selO[jj] = psel(n & 7, 1, jj); }
    if (st1) {
      const unsigned tagw = ep + 1u, tag = tagw & 0xffu;
      ln_slice(xr, lane, i, &L.xn[a][0][0]);
      v2u xw[5];
#pragma unroll
      for (int u = 0; u < 5; ++u) xw[u] = *reinterpret_cast<const v2u*>(&L.xn[a][n >> 3][20 * g + 4 * u]);
      f32x4 d0 = {0.f, 0.f, 0.f, 0.f}, d1 = {0.f, 0.f, 0.f, 0.f};
      if (a == 0) {
#pragma unroll
        for (int u = 0; u < 10; ++u) { const unsigned src = (u & 1) ? xw[u >> 1].y : xw[u >> 1].x; d0 = mfma32(W[2 * u], perm4(src, selE), d0); d1 = mfma32(W[2 * u + 1], perm4(src, selO), d1); }
        d0 += d1;
      } else {
#pragma unroll
        for (int u = 0; u < 10; ++u) { const unsigned src = (u & 1) ? xw[u >> 1].y : xw[u >> 1].x; d0 = mfma32(W[u], perm4(src, selE), d0); d1 = mfma32(W[u], perm4(src, selO), d1); }
#pragma unroll
        for (int k = 0; k < 4; ++k) d0[k] = x32_sum(g < 2 ? d0[k] : d1[k]);
      }
      const int po = a ? WS_HP + (gi * 8 + i) * 512 + (n >> 3) * 256 : WS_QP + (h * 8 + i) * 1024 + (n >> 3) * 512;
      if (!a || g < 2) st_unit(rws, po + (g * 8 + (n & 7)) * 16, d0, tag);
      __builtin_amdgcn_s_sleep(GATHER_SLEEP);
      const int gs = a ? 512 : 1024;
      const int go = (a ? WS_HP + gi * 4096 : WS_QP + h * 8192 + b * 512) + (lane >> 5) * gs + (lane & 31) * 16;
      v4u G[4];
      for (unsigned spin = 0;; ++spin) {
        MEMBAR();
#pragma unroll
        for (int p = 0; p < 4; ++p) G[p] = ld_sc1(rws, go, p * 2 * gs);
        const unsigned bad = unit_bad(G[0], tag) | unit_bad(G[1], tag) | unit_bad(G[2], tag) | unit_bad(G[3], tag);
        if (__all((bad & 0xffu) == 0u) || spin > SPIN_CAP) break;
        __builtin_amdgcn_s_sleep(8);
      }
      f32x4 qv = (unit_val(G[0]) + unit_val(G[1])) + (unit_val(G[2]) + unit_val(G[3]));
#pragma unroll
      for (int k = 0; k < 4; ++k) { qv[k] = x32_sum(qv[k]); if (a) qv[k] = fmaxf(qv[k], 0.f); }
      const unsigned q01 = cvtpk(qv[0], qv[1]), q23 = cvtpk(qv[2], qv[3]);
      if (lane < 32) {
        unsigned short* dst = &L.qh[a][(lane >> 3) * 32 + (lane & 7)];
        dst[0] = (unsigned short)q01; dst[8] = (unsigned short)(q01 >> 16); dst[16] = (unsigned short)q23; dst[24] = (unsigned short)(q23 >> 16);
      }
      if (tid == 256) L.tag = tagw;
    }
    wg_barrier();
    const unsigned tagw = L.tag, tag = tagw & 0xffu;
    const v2u hw = *reinterpret_cast<const v2u*>(&L.qh[1][(n >> 3) * 64 + 16 * a + 4 * g]);
    const int ocol = 8 * (4 * (gi >> 3) + ((lane >> 1) & 3)) + 2 * (gi & 3) + (lane & 1);
    const int oidx = ((gi >> 2) & 1) * D + i * 80 + ocol;
    const bool ow_ = a == 0 && gi < 24 && lane < 8 && ocol < 80;
    unsigned xres = 0;
    if (ow_) xres = x[oidx];
    if (a == 2 && lane < 40) st_unit(rws, WS_AP + (h * 8 + i) * 640 + lane * 16, f32x4{0.f, 0.f, 0.f, 0.f}, tag ^ 0x80u);
    const rsrc_t r2 = mkrsrc(w2, DFF * D * 2), ro = mkrsrc(wo, H * DH * D * 2);
    v4u W2[4], WO[8];
    const int v2o = (n < 10 && ffn) ? (64 * gi + 16 * a + 4 * g) * 1280 + i * 160 + n * 16 : OOB;
    const int voo = n < 10 ? (128 * h + 32 * a + 2 * g) * 1280 + i * 160 + n * 16 : OOB;
    VMCNT(0);
    wg_barrier();
#pragma unroll
    for (int t = 0; t < 4; ++t) W2[t] = ld_nt(r2, v2o, t * 1280);
#pragma unroll
    for (int t = 0; t < 8; ++t) WO[t] = ld_nt(ro, voo, (8 * (t & 3) + (t >> 2)) * 1280);
    SB();
    wg_barrier();
    f32x4 df = {0.f, 0.f, 0.f, 0.f};
    df = mfma32(W2[0], perm4(hw.x, selE), df); df = mfma32(W2[1], perm4(hw.x, selO), df);
    df = mfma32(W2[2], perm4(hw.y, selE), df); df = mfma32(W2[3], perm4(hw.y, selO), df);
    L.df[a][lane] = df;
    wg_barrier();
    const int apo = WS_AP + (h * 8 + (n >> 3) * 4 + (n & 3)) * 640;
    const int auo = apo + (8 * a + 2 * g + ((n >> 2) & 1)) * 16;
    v4u AU = {0, 0, 0, 0}, AM = {0, 0, 0, 0};
    __builtin_amdgcn_s_sleep(P1FIRST);
    AU = __builtin_amdgcn_raw_buffer_load_b128(rws, auo, 0, AUX_SYS); AM = __builtin_amdgcn_raw_buffer_load_b128(rws, apo, 512, AUX_SYS);
    SB();
    {
      bool nu = ((unit_bad(AU, tag) | unit_bad(AM, tag)) & 0xffu) != 0u;
      for (unsigned spin = 0; __any(nu) && spin <= SPIN_CAP; ++spin) {
        __builtin_amdgcn_s_sleep(P1SLEEP);
        MEMBAR();
        const v4u u_ = __builtin_amdgcn_raw_buffer_load_b128(rws, nu ? auo : OOB, 0, AUX_SYS), m_ = __builtin_amdgcn_raw_buffer_load_b128(rws, nu ? apo : OOB, 512, AUX_SYS);
        if (nu) { AU = u_; AM = m_; }
        nu = nu && ((unit_bad(u_, tag) | unit_bad(m_, tag)) & 0xffu) != 0u;
      }
    }
    {
      const f32x4 am = unit_val(AM);
      float M = am[0]; M = fmaxf(M, dppf<XOR1>(M)); M = fmaxf(M, dppf<XOR2>(M));
      const float w = __builtin_amdgcn_exp2f((am[0] - M) * LOG2E);
      float Ls = w * am[1]; Ls += dppf<XOR1>(Ls); Ls += dppf<XOR2>(Ls);
      f32x4 o = unit_val(AU) * (w * __builtin_amdgcn_rcpf(Ls));
#pragma unroll
      for (int k = 0; k < 4; ++k) o[k] += dppf<XOR1>(o[k]);
#pragma unroll
      for (int k = 0; k < 4; ++k) o[k] += dppf<XOR2>(o[k]);
      const unsigned m01 = cvtpk(o[0], o[1]), m23 = cvtpk(o[2], o[3]), t01 = dppu<HMIR>(m01), t23 = dppu<HMIR>(m23);
      const bool hf = (n & 4) != 0;
      const unsigned ow[4] = {hf ? t01 : m01, hf ? t23 : m23, hf ? m01 : t01, hf ? m23 : t23};
      f32x4 ds = {0.f, 0.f, 0.f, 0.f}, dt = {0.f, 0.f, 0.f, 0.f};
#pragma unroll
      for (int t = 0; t < 4; ++t) { ds = mfma32(WO[2 * t], perm4(ow[t], selE), ds); dt = mfma32(WO[2 * t + 1], perm4(ow[t], selO), dt); }
      if (lane < 48) st_unit(rws, WS_SP + ((i * 32 + h) * 24 + (lane >> 1)) * 128 + a * 32 + (lane & 1) * 16, ds + dt, tag);
    }
    if (a == 0 && gi < 24) {
      const int p = lane >> 1;
      const int so = WS_SP + ((i * 32 + (lane >> 3)) * 24 + gi) * 128 + (lane & 7) * 16;
      const int fo = p < NG ? WS_FP + (i * 32 + p) * 768 + gi * 32 + (lane & 1) * 16 : OOB;
      v4u S[4], F0;
      __builtin_amdgcn_s_sleep(P2FIRST);
      for (unsigned spin = 0;; ++spin) {
        MEMBAR();
#pragma unroll
        for (int q = 0; q < 4; ++q) S[q] = __builtin_amdgcn_raw_buffer_load_b128(rws, so, q * (8 * 24 * 128), AUX_SYS);
        F0 = __builtin_amdgcn_raw_buffer_load_b128(rws, fo, 0, AUX_SYS);
        const unsigned bad = unit_bad(S[0], tag) | unit_bad(S[1], tag) | unit_bad(S[2], tag) | unit_bad(S[3], tag) | (p < NG ? unit_bad(F0, tag) : 0u);
        if (__all((bad & 0xffu) == 0u) || spin > SPIN_CAP) break;
        __builtin_amdgcn_s_sleep(P2SLEEP);
      }
      const f32x4 rs = (unit_val(S[0]) + unit_val(S[1])) + (unit_val(S[2]) + unit_val(S[3])), rf = unit_val(F0);
      float s2[2], f2[2];
#pragma unroll
      for (int k = 0; k < 2; ++k) {
        const float a0 = rs[k] + dppf<ROR4>(rs[k]), a1 = rs[k + 2] + dppf<ROR4>(rs[k + 2]), b0 = rf[k] + dppf<ROR4>(rf[k]), b1 = rf[k + 2] + dppf<ROR4>(rf[k + 2]);
        const float a0r = a0 + dppf<ROR8>(a0), a1r = a1 + dppf<ROR8>(a1), b0r = b0 + dppf<ROR8>(b0), b1r = b1 + dppf<ROR8>(b1);
        s2[k] = (lane & 4) ? a1r : a0r; f2[k] = (lane & 4) ? b1r : b0r;
      }
      const float sa = s2[0] + dppf<XOR2>(s2[0]), sb = s2[1] + dppf<XOR2>(s2[1]), fa = f2[0] + dppf<XOR2>(f2[0]), fb = f2[1] + dppf<XOR2>(f2[1]);
      const float sv = rbf(x32_sum(x16_sum((lane & 2) ? sb : sa))), fv = rbf(x32_sum(x16_sum((lane & 2) ? fb : fa)));
      const float gv = rbf(fv * __builtin_amdgcn_rcpf(1.0f + __builtin_amdgcn_exp2f(-fv * LOG2E)));
      const float o = bflo(xres) + rbf(sv * gv);
      if (ow_) out[oidx] = (unsigned short)cvtpk(o, o);
      if (gi == 0 && i == 0 && lane == 0) __builtin_amdgcn_raw_buffer_store_b32(tagw, rws, WS_EPOCH, 0, AUX_SC1);
    }
  }
}
}

extern "C" void kernel_launch(void* const* d_in, const int* in_sizes, int n_in, void* d_out, int out_size, void* d_ws, size_t ws_size, hipStream_t stream) {
  fused_decode<<<256, 512, 0, stream>>>((const unsigned short*)d_in[0], (const unsigned short*)d_in[1], (const unsigned short*)d_in[2],
                                        (const unsigned short*)d_in[3], (const unsigned short*)d_in[4], (const unsigned short*)d_in[6],
                                        (const unsigned short*)d_in[7], (unsigned short*)d_out, (unsigned char*)d_ws);
}
```

```cpp
#include <hip/hip_runtime.h>
#include <hip/hip_bf16.h>
#include <stdint.h>

namespace {
typedef unsigned v4u __attribute__((ext_vector_type(4)));
typedef unsigned v2u __attribute__((ext_vector_type(2)));
typedef float f32x4 __attribute__((ext_vector_type(4)));
typedef __bf16 bf16x2 __attribute__((ext_vector_type(2)));
typedef __bf16 bf16x8 __attribute__((ext_vector_type(8)));
typedef __amdgpu_buffer_rsrc_t rsrc_t;

constexpr int D = 640, H = 32, SK = 8192, DH = 128, WIN = 2048, DFF = 1728, NG = 27;
constexpr int AUX_NT = 2, AUX_SC1 = 16, RSRC3 = 0x00020000;
constexpr int OOB = 0x7ffffff0;
constexpr int WS_EPOCH = 0;
constexpr int WS_QP = 4096;
constexpr int WS_HP = WS_QP + 32 * 8 * 1024;
constexpr int WS_AP = WS_HP + 32 * 8 * 512;
constexpr int WS_SP = WS_AP + 32 * 8 * 640;
constexpr int WS_FP = WS_SP + 8 * 32 * 24 * 128;
constexpr int WS_END = WS_FP + 8 * 32 * 768;
constexpr unsigned SPIN_CAP = 1u << 18;
constexpr float LOG2E = 1.4426950408889634f;
constexpr int KWIN = 12, KPRE = 2, KDELAY = 10, VPRE = 20, VRING = 22;
constexpr int GATHER_SLEEP = 40, P1FIRST = 16, P1SLEEP = 2, P2FIRST = 20, P2SLEEP = 2;
constexpr unsigned PACE0 = 40, PACE = 15;
constexpr int AUX_SYS = 17;

#define SB() __builtin_amdgcn_sched_barrier(0)
#define MEMBAR() asm volatile("" ::: "memory")
#define VMCNT(n) asm volatile("s_waitcnt vmcnt(%0)" ::"n"(n) : "memory")
__device__ __forceinline__ void wg_barrier() { SB(); asm volatile("s_waitcnt lgkmcnt(0)\n\ts_barrier" ::: "memory"); SB(); }
__device__ __forceinline__ float bflo(unsigned u) { return __uint_as_float(u << 16); }
__device__ __forceinline__ float bfhi(unsigned u) { return __uint_as_float(u & 0xffff0000u); }
__device__ __forceinline__ unsigned cvtpk(float lo, float hi) { bf16x2 v = {(__bf16)lo, (__bf16)hi}; return __builtin_bit_cast(unsigned, v); }
__device__ __forceinline__ float rbf(float f) { return __uint_as_float(cvtpk(f, f) & 0xffff0000u); }
template <int CTRL> __device__ __forceinline__ float dppf(float x) {
  return __builtin_bit_cast(float, __builtin_amdgcn_mov_dpp(__builtin_bit_cast(int, x), CTRL, 0xf, 0xf, true));
}
template <int CTRL> __device__ __forceinline__ unsigned dppu(unsigned x) { return (unsigned)__builtin_amdgcn_mov_dpp((int)x, CTRL, 0xf, 0xf, true); }
constexpr int XOR1 = 0xB1, XOR2 = 0x4E, HMIR = 0x141, ROR8 = 0x128, ROR4 = 0x124, NEWB = 0x150;
__device__ __forceinline__ float x16_sum(float x) { auto s = __builtin_amdgcn_permlane16_swap(__float_as_uint(x), __float_as_uint(x), false, false); return __uint_as_float(s[0]) + __uint_as_float(s[1]); }
__device__ __forceinline__ float x32_sum(float x) { auto s = __builtin_amdgcn_permlane32_swap(__float_as_uint(x), __float_as_uint(x), false, false); return __uint_as_float(s[0]) + __uint_as_float(s[1]); }
__device__ __forceinline__ float x16_max(float x) { auto s = __builtin_amdgcn_permlane16_swap(__float_as_uint(x), __float_as_uint(x), false, false); return fmaxf(__uint_as_float(s[0]), __uint_as_float(s[1])); }
__device__ __forceinline__ float x32_max(float x) { auto s = __builtin_amdgcn_permlane32_swap(__float_as_uint(x), __float_as_uint(x), false, false); return fmaxf(__uint_as_float(s[0]), __uint_as_float(s[1])); }
__device__ __forceinline__ float row_sum(float x) { x += dppf<XOR1>(x); x += dppf<XOR2>(x); x += dppf<HMIR>(x); x += dppf<ROR8>(x); return x; }
__device__ __forceinline__ float row_max(float x) { x = fmaxf(x, dppf<XOR1>(x)); x = fmaxf(x, dppf<XOR2>(x)); x = fmaxf(x, dppf<HMIR>(x)); x = fmaxf(x, dppf<ROR8>(x)); return x; }
__device__ __forceinline__ float dot2(unsigned k, unsigned q, float acc) {
  return __builtin_amdgcn_fdot2_f32_bf16(__builtin_bit_cast(bf16x2, k), __builtin_bit_cast(bf16x2, q), acc, false);
}
__device__ __forceinline__ f32x4 mfma32(v4u a, v4u b, f32x4 c) {
  return __builtin_amdgcn_mfma_f32_16x16x32_bf16(__builtin_bit_cast(bf16x8, a), __builtin_bit_cast(bf16x8, b), c, 0, 0, 0);
}
__device__ __forceinline__ rsrc_t mkrsrc(const void* p, unsigned bytes) { return __builtin_amdgcn_make_buffer_rsrc((void*)p, 0, bytes, RSRC3); }
__device__ __forceinline__ v4u ld_nt(rsrc_t r, int voff, int soff) { return __builtin_amdgcn_raw_buffer_load_b128(r, voff, soff, AUX_NT); }
__device__ __forceinline__ v4u ld_sc1(rsrc_t r, int voff, int soff) { return __builtin_amdgcn_raw_buffer_load_b128(r, voff, soff, AUX_SC1); }
struct Tag { unsigned b[4]; };
__device__ __forceinline__ Tag make_tag(unsigned epoch1, unsigned flip = 0u) {
  const unsigned t = (unsigned)__builtin_amdgcn_readfirstlane(epoch1) * 0x9E3779B1u ^ flip;
  return Tag{{t & 0xffu, (t >> 8) & 0xffu, (t >> 16) & 0xffu, t >> 24}};
}
__device__ __forceinline__ void st_unit(rsrc_t r, int voff, f32x4 v, const Tag& tag) {
  v4u u;
#pragma unroll
  for (int k = 0; k < 4; ++k) u[k] = (__float_as_uint(v[k]) & 0xffffff00u) | tag.b[k];
  __builtin_amdgcn_raw_buffer_store_b128(u, r, voff, 0, AUX_SC1);
}
__device__ __forceinline__ unsigned unit_bad(v4u g, const Tag& tag) { return (g.x ^ tag.b[0]) | (g.y ^ tag.b[1]) | (g.z ^ tag.b[2]) | (g.w ^ tag.b[3]); }
__device__ __forceinline__ f32x4 unit_val(v4u g) {
  return f32x4{__uint_as_float(g.x & 0xffffff00u), __uint_as_float(g.y & 0xffffff00u), __uint_as_float(g.z & 0xffffff00u), __uint_as_float(g.w & 0xffffff00u)};
}
__device__ __forceinline__ unsigned psel(int e, int sh, int jj) {
  const unsigned pair = (unsigned)(2 * sh) | ((unsigned)(2 * sh + 1) << 8);
  const unsigned s = (e & 1) ? ((pair << 16) | 0x0c0cu) : (0x0c0c0000u | pair);
  return (e >> 1) == jj ? s : 0x0c0c0c0cu;
}
__device__ __forceinline__ unsigned perm(unsigned src, unsigned sel) { return __builtin_amdgcn_perm(src, src, sel); }
__device__ __forceinline__ v4u perm4(unsigned src, const unsigned (&s)[4]) { return v4u{perm(src, s[0]), perm(src, s[1]), perm(src, s[2]), perm(src, s[3])}; }

struct __attribute__((aligned(16))) Lds {
  f32x4 df[4][64];
  f32x4 acc[4][32];
  float ml[8];
  unsigned short xn[2][2][80];
  unsigned short qh[2][128];
  unsigned tag; unsigned pad[3];
};

struct XRegs { v4u c[3]; };
__device__ __forceinline__ void x_issue(XRegs& xr, rsrc_t rx, int lane) {
  const int vo = (lane >> 5) * 1280 + (lane & 31) * 16;
  xr.c[0] = __builtin_amdgcn_raw_buffer_load_b128(rx, vo, 0, 0);
  xr.c[1] = __builtin_amdgcn_raw_buffer_load_b128(rx, vo, 512, 0);
  xr.c[2] = __builtin_amdgcn_raw_buffer_load_b128(rx, (lane & 16) ? OOB : vo, 1024, 0);
}
__device__ __forceinline__ void ln_slice(const XRegs& xr, int lane, int i, unsigned short* dst) {
  float sa = 0.f, sb = 0.f, sc = 0.f;
#pragma unroll
  for (int k = 0; k < 4; ++k) { sa = dot2(xr.c[0][k], 0x3f803f80u, sa); sb = dot2(xr.c[1][k], 0x3f803f80u, sb); sc = dot2(xr.c[2][k], 0x3f803f80u, sc); }
  const float mean = x16_sum(row_sum(sa + sb + sc)) * (1.0f / D);
  float q0 = 0.f, q1 = 0.f, q2 = 0.f, q3 = 0.f, q4 = 0.f, q5 = 0.f;
#pragma unroll
  for (int k = 0; k < 4; ++k) {
    const float a0 = bflo(xr.c[0][k]) - mean, a1 = bfhi(xr.c[0][k]) - mean, b0 = bflo(xr.c[1][k]) - mean, b1 = bfhi(xr.c[1][k]) - mean;
    const float c0 = bflo(xr.c[2][k]) - mean, c1 = bfhi(xr.c[2][k]) - mean;
    q0 = fmaf(a0, a0, q0); q1 = fmaf(a1, a1, q1); q2 = fmaf(b0, b0, q2); q3 = fmaf(b1, b1, q3); q4 = fmaf(c0, c0, q4); q5 = fmaf(c1, c1, q5);
  }
  float ss = (q0 + q1) + (q2 + q3) + ((lane & 16) ? 0.f : q4 + q5);
  ss = x16_sum(row_sum(ss));
  const float mb = rbf(mean), den = rbf(__builtin_amdgcn_sqrtf(rbf(rbf(ss * (1.0f / D)) + 1e-5f)));
  const float rden = __builtin_amdgcn_rcpf(den);
  const int rel = ((lane & 31) - 10 * i) & 31;
  const int cidx = 10 * i + rel;
  const v4u m = cidx < 32 ? xr.c[0] : cidx < 64 ? xr.c[1] : xr.c[2];
  v4u o;
#pragma unroll
  for (int k = 0; k < 4; ++k) { const unsigned d = cvtpk(bflo(m[k]) - mb, bfhi(m[k]) - mb); o[k] = cvtpk(bflo(d) * rden, bfhi(d) * rden); }
  if (rel < 10) *reinterpret_cast<v4u*>(dst + (lane >> 5) * 80 + rel * 8) = o;
  asm volatile("s_waitcnt lgkmcnt(0)" ::: "memory");
}

__global__ void __launch_bounds__(512, 2) __attribute__((aligned(4096)))
fused_decode(const unsigned short* __restrict__ x, const unsigned short* __restrict__ kc, const unsigned short* __restrict__ vc,
             const unsigned short* __restrict__ wq, const unsigned short* __restrict__ wo, const unsigned short* __restrict__ w1,
             const unsigned short* __restrict__ w2, unsigned short* __restrict__ out, unsigned char* __restrict__ ws) {
  __shared__ Lds L;
  const int tid = threadIdx.x, lane = tid & 63, wave = __builtin_amdgcn_readfirstlane(tid >> 6);
  const int bid = blockIdx.x, xcd = bid & 7, sl = bid >> 3, kq = sl >> 3, h = xcd * 4 + kq, i = sl & 7, b = i >> 2, j = i & 3;
  const int gi = kq * 8 + xcd;
  const bool ffn = gi < NG;
  const int g = lane >> 4, n = lane & 15;
  const rsrc_t rws = mkrsrc(ws, WS_END);
  const unsigned t_start = (unsigned)__builtin_amdgcn_s_memrealtime();
  asm volatile("" :: "s"(x), "s"(kc), "s"(vc), "s"(wq), "s"(wo), "s"(w1), "s"(w2), "s"(out), "s"(ws));
  const size_t kvbase = ((size_t)(b * H + h) * SK + (SK - WIN) + j * 512) * DH;
  const rsrc_t rk = mkrsrc(kc + kvbase, 131072);
  const int lo = wave * 1024 + lane * 16;
  v4u K[32];
  const int a = wave - 4;
  const bool st1 = a == 0 || (a == 1 && ffn);
  unsigned ep = 0; XRegs xr; v4u W[20];
  if (st1) {
    __builtin_amdgcn_s_setprio(3);
    const rsrc_t rx = mkrsrc(x, 2 * D * 2);
    x_issue(xr, rx, lane);
    ep = __builtin_amdgcn_raw_buffer_load_b32(rws, WS_EPOCH, 0, AUX_SC1);
    if (a == 0) {
      const rsrc_t rw = mkrsrc(wq, D * H * DH * 2);
      const int vo = (80 * i + 20 * g) * 8192 + h * 256 + n * 16;
#pragma unroll
      for (int t = 0; t < 20; ++t) W[t] = ld_nt(rw, vo, t * 8192);
    } else {
      const rsrc_t rw = mkrsrc(w1, D * DFF * 2);
      const int vo = (80 * i + 20 * g + (n >> 3)) * (DFF * 2) + gi * 128 + (n & 7) * 16;
#pragma unroll
      for (int u = 0; u < 10; ++u) W[u] = ld_nt(rw, vo, u * (DFF * 4));
    }
    SB();
  } else if (wave < 4) {
#pragma unroll
    for (int t = 0; t < KPRE; ++t) K[t] = ld_nt(rk, lo, t * 4096);
  }
  int wave2 = wave;
  asm volatile("" : "+s"(wave2));
  SB();
  if (__builtin_expect(wave2 < 4, 1)) {
    const rsrc_t rv = mkrsrc(vc + kvbase, 131072);
    v4u V[32];
    __builtin_amdgcn_s_sleep(KDELAY);
#define PACE_WAIT(t) if (((t) & 3) == 0 && (t) >= KWIN) { const unsigned tgt_ = t_start + PACE0 + (t) * PACE; \
      while ((int)((unsigned)__builtin_amdgcn_s_memrealtime() - tgt_) < 0) __builtin_amdgcn_s_sleep(1); SB(); }
#pragma unroll
    for (int t = KPRE; t < 32; ++t) { if (t >= KWIN) VMCNT(KWIN - 1); PACE_WAIT(t) K[t] = ld_nt(rk, lo, t * 4096); SB(); }
#pragma unroll
    for (int t = 0; t < VPRE; ++t) { VMCNT(KWIN - 1); PACE_WAIT(32 + t) V[t] = ld_nt(rv, lo, t * 4096); SB(); }
    unsigned sel[4];
#pragma unroll
    for (int jj = 0; jj < 4; ++jj) sel[jj] = psel(n & 7, n >> 3, jj);
    wg_barrier();
#pragma unroll
    for (int t = VPRE; t < VRING; ++t) { V[t] = ld_nt(rv, lo, t * 4096); SB(); }
    const v4u qv = *reinterpret_cast<const v4u*>(&L.qh[0][n * 8]);
    const Tag tag = make_tag(L.tag);
    float xs[32];
#pragma unroll
    for (int t = 0; t < 32; ++t) { float d = dot2(K[t].x, qv.x, 0.f); d = dot2(K[t].y, qv.y, d); d = dot2(K[t].z, qv.z, d); xs[t] = dot2(K[t].w, qv.w, d); }
#define TR_STEP(HALF, CTRL, BIT) _Pragma("unroll") for (int u = 0; u < (HALF); ++u) { \
      const float lo_ = xs[u], hi_ = xs[u + (HALF)]; const float t1_ = lo_ + dppf<CTRL>(lo_), t2_ = hi_ + dppf<CTRL>(hi_); xs[u] = (n & (BIT)) ? t2_ : t1_; }
    TR_STEP(16, ROR8, 8) TR_STEP(8, HMIR, 4) TR_STEP(4, XOR2, 2) TR_STEP(2, XOR1, 1)
#undef TR_STEP
    const float s0 = rbf(rbf(xs[0]) * 0.08838834764831845f), s1 = rbf(rbf(xs[1]) * 0.08838834764831845f);
    const float m = x32_max(x16_max(row_max(fmaxf(s0, s1))));
    const float p0 = __builtin_amdgcn_exp2f((s0 - m) * LOG2E), p1 = __builtin_amdgcn_exp2f((s1 - m) * LOG2E);
    const unsigned P0 = cvtpk(p0, p0 - rbf(p0)), P1 = cvtpk(p1, p1 - rbf(p1));
    const float lsum = x32_sum(x16_sum(row_sum(p0 + p1)));
    if (lane == 0) { L.ml[wave] = m; L.ml[4 + wave] = lsum; }
    f32x4 acc = {0.f, 0.f, 0.f, 0.f}, fw = {0.f, 0.f, 0.f, 0.f};
    float M = 0.f, Ls = 0.f;
#define PV(t) { const unsigned bc = dppu<NEWB + ((t) >> 1)>(((t) & 1) ? P1 : P0); acc = mfma32(V[t], perm4(bc, sel), acc); \
      if ((t) + VRING < 32) { PACE_WAIT(32 + VRING + (t)) V[((t) + VRING) & 31] = ld_nt(rv, lo, ((t) + VRING) * 4096); } SB(); \
      if ((t) + VRING == 31) { wg_barrier();     \
        const f32x4 mm = *reinterpret_cast<const f32x4*>(&L.ml[0]), ll = *reinterpret_cast<const f32x4*>(&L.ml[4]); \
        M = fmaxf(fmaxf(mm[0], mm[1]), fmaxf(mm[2], mm[3])); \
        _Pragma("unroll") for (int w = 0; w < 4; ++w) { fw[w] = __builtin_amdgcn_exp2f((mm[w] - M) * LOG2E); Ls = fmaf(fw[w], ll[w], Ls); } \
        asm volatile("" : "+v"(M), "+v"(Ls), "+v"(fw)); SB(); } }
    PV(0) PV(1) PV(2) PV(3) PV(4) PV(5) PV(6) PV(7) PV(8) PV(9) PV(10) PV(11) PV(12) PV(13) PV(14) PV(15)
    PV(16) PV(17) PV(18) PV(19) PV(20) PV(21) PV(22) PV(23) PV(24) PV(25) PV(26) PV(27) PV(28) PV(29) PV(30) PV(31)
#undef PV
#undef PACE_WAIT
#pragma unroll
    for (int k = 0; k < 4; ++k) acc[k] += dppf<ROR8>(acc[k]);
    if (n < 8) L.acc[wave][g * 8 + n] = acc;
    wg_barrier();
    if (wave == 0) {
      f32x4 av[4];
#pragma unroll
      for (int w = 0; w < 4; ++w) av[w] = L.acc[w][lane & 31];
      f32x4 v = (fw[0] * av[0] + fw[1] * av[1]) + (fw[2] * av[2] + fw[3] * av[3]);
      if (lane == 32) v = f32x4{M, Ls, M, Ls};
      if (lane <= 32) st_unit(rws, WS_AP + (h * 8 + i) * 640 + lane * 16, v, tag);
    }
    wg_barrier();
    if (wave == 1 && lane < 48 && ffn) {
      const f32x4* src = &L.df[0][lane];
      st_unit(rws, WS_FP + (i * 32 + gi) * 768 + lane * 16, (src[0] + src[64]) + (src[128] + src[192]), tag);
    }
  } else {
    __builtin_amdgcn_s_setprio(3);
    unsigned selE[4], selO[4];
#pragma unroll
    for (int jj = 0; jj < 4; ++jj) { selE[jj] = psel(n & 7, 0, jj); selO[jj] = psel(n & 7, 1, jj); }
    if (st1) {
      const unsigned tagw = ep + 1u;
      const Tag tag = make_tag(tagw);
      ln_slice(xr, lane, i, &L.xn[a][0][0]);
      v2u xw[5];
#pragma unroll
      for (int u = 0; u < 5; ++u) xw[u] = *reinterpret_cast<const v2u*>(&L.xn[a][n >> 3][20 * g + 4 * u]);
      f32x4 d0 = {0.f, 0.f, 0.f, 0.f}, d1 = {0.f, 0.f, 0.f, 0.f};
      if (a == 0) {
#pragma unroll
        for (int u = 0; u < 10; ++u) { const unsigned src = (u & 1) ? xw[u >> 1].y : xw[u >> 1].x; d0 = mfma32(W[2 * u], perm4(src, selE), d0); d1 = mfma32(W[2 * u + 1], perm4(src, selO), d1); }
        d0 += d1;
      } else {
#pragma unroll
        for (int u = 0; u < 10; ++u) { const unsigned src = (u & 1) ? xw[u >> 1].y : xw[u >> 1].x; d0 = mfma32(W[u], perm4(src, selE), d0); d1 = mfma32(W[u], perm4(src, selO), d1); }
#pragma unroll
        for (int k = 0; k < 4; ++k) d0[k] = x32_sum(g < 2 ? d0[k] : d1[k]);
      }
      const int po = a ? WS_HP + (gi * 8 + i) * 512 + (n >> 3) * 256 : WS_QP + (h * 8 + i) * 1024 + (n >> 3) * 512;
      if (!a || g < 2) st_unit(rws, po + (g * 8 + (n & 7)) * 16, d0, tag);
      __builtin_amdgcn_s_sleep(GATHER_SLEEP);
      const int gs = a ? 512 : 1024;
      const int go = (a ? WS_HP + gi * 4096 : WS_QP + h * 8192 + b * 512) + (lane >> 5) * gs + (lane & 31) * 16;
      v4u G[4];
      for (unsigned spin = 0;; ++spin) {
        MEMBAR();
#pragma unroll
        for (int p = 0; p < 4; ++p) G[p] = ld_sc1(rws, go, p * 2 * gs);
        const unsigned bad = unit_bad(G[0], tag) | unit_bad(G[1], tag) | unit_bad(G[2], tag) | unit_bad(G[3], tag);
        if (__all((bad & 0xffu) == 0u) || spin > SPIN_CAP) break;
        __builtin_amdgcn_s_sleep(8);
      }
      f32x4 qv = (unit_val(G[0]) + unit_val(G[1])) + (unit_val(G[2]) + unit_val(G[3]));
#pragma unroll
      for (int k = 0; k < 4; ++k) { qv[k] = x32_sum(qv[k]); if (a) qv[k] = fmaxf(qv[k], 0.f); }
      const unsigned q01 = cvtpk(qv[0], qv[1]), q23 = cvtpk(qv[2], qv[3]);
      if (lane < 32) {
        unsigned short* dst = &L.qh[a][(lane >> 3) * 32 + (lane & 7)];
        dst[0] = (unsigned short)q01; dst[8] = (unsigned short)(q01 >> 16); dst[16] = (unsigned short)q23; dst[24] = (unsigned short)(q23 >> 16);
      }
      if (tid == 256) L.tag = tagw;
    }
    wg_barrier();
    const unsigned tagw = L.tag;
    const Tag tag = make_tag(tagw);
    const v2u hw = *reinterpret_cast<const v2u*>(&L.qh[1][(n >> 3) * 64 + 16 * a + 4 * g]);
    const int ocol = 8 * (4 * (gi >> 3) + ((lane >> 1) & 3)) + 2 * (gi & 3) + (lane & 1);
    const int oidx = ((gi >> 2) & 1) * D + i * 80 + ocol;
    const bool ow_ = a == 0 && gi < 24 && lane < 8 && ocol < 80;
    unsigned xres = 0;
    if (ow_) xres = x[oidx];
    if (a == 2 && lane < 40) st_unit(rws, WS_AP + (h * 8 + i) * 640 + lane * 16, f32x4{0.f, 0.f, 0.f, 0.f}, make_tag(tagw, 0x80808080u));
    const rsrc_t r2 = mkrsrc(w2, DFF * D * 2), ro = mkrsrc(wo, H * DH * D * 2);
    v4u W2[4], WO[8];
    const int v2o = (n < 10 && ffn) ? (64 * gi + 16 * a + 4 * g) * 1280 + i * 160 + n * 16 : OOB;
    const int voo = n < 10 ? (128 * h + 32 * a + 2 * g) * 1280 + i * 160 + n * 16 : OOB;
    VMCNT(0);
    wg_barrier();
#pragma unroll
    for (int t = 0; t < 4; ++t) W2[t] = ld_nt(r2, v2o, t * 1280);
#pragma unroll
    for (int t = 0; t < 8; ++t) WO[t] = ld_nt(ro, voo, (8 * (t & 3) + (t >> 2)) * 1280);
    SB();
    wg_barrier();
    f32x4 df = {0.f, 0.f, 0.f, 0.f};
    df = mfma32(W2[0], perm4(hw.x, selE), df); df = mfma32(W2[1], perm4(hw.x, selO), df);
    df = mfma32(W2[2], perm4(hw.y, selE), df); df = mfma32(W2[3], perm4(hw.y, selO), df);
    L.df[a][lane] = df;
    wg_barrier();
    const int apo = WS_AP + (h * 8 + (n >> 3) * 4 + (n & 3)) * 640;
    const int auo = apo + (8 * a + 2 * g + ((n >> 2) & 1)) * 16;
    v4u AU = {0, 0, 0, 0}, AM = {0, 0, 0, 0};
    __builtin_amdgcn_s_sleep(P1FIRST);
    AU = __builtin_amdgcn_raw_buffer_load_b128(rws, auo, 0, AUX_SYS); AM = __builtin_amdgcn_raw_buffer_load_b128(rws, apo, 512, AUX_SYS);
    SB();
    {
      bool nu = ((unit_bad(AU, tag) | unit_bad(AM, tag)) & 0xffu) != 0u;
      for (unsigned spin = 0; __any(nu) && spin <= SPIN_CAP; ++spin) {
        __builtin_amdgcn_s_sleep(P1SLEEP);
        MEMBAR();
        const v4u u_ = __builtin_amdgcn_raw_buffer_load_b128(rws, nu ? auo : OOB, 0, AUX_SYS), m_ = __builtin_amdgcn_raw_buffer_load_b128(rws, nu ? apo : OOB, 512, AUX_SYS);
        if (nu) { AU = u_; AM = m_; }
        nu = nu && ((unit_bad(u_, tag) | unit_bad(m_, tag)) & 0xffu) != 0u;
      }
    }
    {
      const f32x4 am = unit_val(AM);
      float M = am[0]; M = fmaxf(M, dppf<XOR1>(M)); M = fmaxf(M, dppf<XOR2>(M));
      const float w = __builtin_amdgcn_exp2f((am[0] - M) * LOG2E);
      float Ls = w * am[1]; Ls += dppf<XOR1>(Ls); Ls += dppf<XOR2>(Ls);
      f32x4 o = unit_val(AU) * (w * __builtin_amdgcn_rcpf(Ls));
#pragma unroll
      for (int k = 0; k < 4; ++k) o[k] += dppf<XOR1>(o[k]);
#pragma unroll
      for (int k = 0; k < 4; ++k) o[k] += dppf<XOR2>(o[k]);
      const unsigned m01 = cvtpk(o[0], o[1]), m23 = cvtpk(o[2], o[3]), t01 = dppu<HMIR>(m01), t23 = dppu<HMIR>(m23);
      const bool hf = (n & 4) != 0;
      const unsigned ow[4] = {hf ? t01 : m01, hf ? t23 : m23, hf ? m01 : t01, hf ? m23 : t23};
      f32x4 ds = {0.f, 0.f, 0.f, 0.f}, dt = {0.f, 0.f, 0.f, 0.f};
#pragma unroll
      for (int t = 0; t < 4; ++t) { ds = mfma32(WO[2 * t], perm4(ow[t], selE), ds); dt = mfma32(WO[2 * t + 1], perm4(ow[t], selO), dt); }
      if (lane < 48) st_unit(rws, WS_SP + ((i * 32 + h) * 24 + (lane >> 1)) * 128 + a * 32 + (lane & 1) * 16, ds + dt, tag);
    }
    if (a == 0 && gi < 24) {
      const int p = lane >> 1;
      const int so = WS_SP + ((i * 32 + (lane >> 3)) * 24 + gi) * 128 + (lane & 7) * 16;
      const int fo = p < NG ? WS_FP + (i * 32 + p) * 768 + gi * 32 + (lane & 1) * 16 : OOB;
      v4u S[4], F0;
      __builtin_amdgcn_s_sleep(P2FIRST);
      for (unsigned spin = 0;; ++spin) {
        MEMBAR();
#pragma unroll
        for (int q = 0; q < 4; ++q) S[q] = __builtin_amdgcn_raw_buffer_load_b128(rws, so, q * (8 * 24 * 128), AUX_SYS);
        F0 = __builtin_amdgcn_raw_buffer_load_b128(rws, fo, 0, AUX_SYS);
        const unsigned bad = unit_bad(S[0], tag) | unit_bad(S[1], tag) | unit_bad(S[2], tag) | unit_bad(S[3], tag) | (p < NG ? unit_bad(F0, tag) : 0u);
        if (__all((bad & 0xffu) == 0u) || spin > SPIN_CAP) break;
        __builtin_amdgcn_s_sleep(P2SLEEP);
      }
      const f32x4 rs = (unit_val(S[0]) + unit_val(S[1])) + (unit_val(S[2]) + unit_val(S[3])), rf = unit_val(F0);
      float s2[2], f2[2];
#pragma unroll
      for (int k = 0; k < 2; ++k) {
        const float a0 = rs[k] + dppf<ROR4>(rs[k]), a1 = rs[k + 2] + dppf<ROR4>(rs[k + 2]), b0 = rf[k] + dppf<ROR4>(rf[k]), b1 = rf[k + 2] + dppf<ROR4>(rf[k + 2]);
        const float a0r = a0 + dppf<ROR8>(a0), a1r = a1 + dppf<ROR8>(a1), b0r = b0 + dppf<ROR8>(b0), b1r = b1 + dppf<ROR8>(b1);
        s2[k] = (lane & 4) ? a1r : a0r; f2[k] = (lane & 4) ? b1r : b0r;
      }
      const float sa = s2[0] + dppf<XOR2>(s2[0]), sb = s2[1] + dppf<XOR2>(s2[1]), fa = f2[0] + dppf<XOR2>(f2[0]), fb = f2[1] + dppf<XOR2>(f2[1]);
      const float sv = rbf(x32_sum(x16_sum((lane & 2) ? sb : sa))), fv = rbf(x32_sum(x16_sum((lane & 2) ? fb : fa)));
      const float gv = rbf(fv * __builtin_amdgcn_rcpf(1.0f + __builtin_amdgcn_exp2f(-fv * LOG2E)));
      const float o = bflo(xres) + rbf(sv * gv);
      if (ow_) out[oidx] = (unsigned short)cvtpk(o, o);
      if (gi == 0 && i == 0 && lane == 0) __builtin_amdgcn_raw_buffer_store_b32(tagw, rws, WS_EPOCH, 0, AUX_SC1);
    }
  }
}
}

extern "C" void kernel_launch(void* const* d_in, const int* in_sizes, int n_in, void* d_out, int out_size, void* d_ws, size_t ws_size, hipStream_t stream) {
  fused_decode<<<256, 512, 0, stream>>>((const unsigned short*)d_in[0], (const unsigned short*)d_in[1], (const unsigned short*)d_in[2],
                                        (const unsigned short*)d_in[3], (const unsigned short*)d_in[4], (const unsigned short*)d_in[6],
                                        (const unsigned short*)d_in[7], (unsigned short*)d_out, (unsigned char*)d_ws);
}
```
